```python
import torch

def _softmax(x, axis=-1):
    return torch.softmax(x, dim=axis)
def _gelu(x):
    return torch.nn.functional.gelu(x, approximate="tanh")
def _silu(x):
    return torch.nn.functional.silu(x)
def _sigmoid(x):
    return torch.sigmoid(x)
def _layernorm(x, axis=-1, eps=1e-5):
    m = x.mean(dim=axis, keepdim=True)
    v = x.var(dim=axis, keepdim=True, unbiased=False)
    return (x - m) / torch.sqrt(v + eps)
def _scatter_add(tgt, idx, src, axis=0):
    return tgt.clone().index_add_(axis, idx, src)
def _segment_sum(src, seg, n):
    out = torch.zeros((n,) + src.shape[1:], dtype=src.dtype)
    return out.index_add_(0, seg, src)
def _pad(x, pw):
    flat = tuple(v for pair in reversed(pw) for v in pair)
    return torch.nn.functional.pad(x, flat)

kernel_name = 'interleave_layers__gqa_block_cached__mixer_token_mlp__decode'

def setup_inputs():

    _w = (lambda a: (lambda t: t.to(torch.bfloat16) if t.is_floating_point() else t)(torch.as_tensor(a)))
    def _A_setup():
        B, Sq, D = 2, 1, 576
        Hq, Hkv, Sk, Dh = 32, 16, 16384, 128
        assert Hq % Hkv == 0, (Hq, Hkv)
        return {
            "x": _w((lambda *s: torch.randn(*s, dtype=torch.bfloat16) * 0.1)(B, Sq, D)),
            "k_cache": _w((lambda *s: torch.randn(*s, dtype=torch.bfloat16) * 0.8)(B, Hkv, Sk, Dh)),
            "v_cache": _w((lambda *s: torch.randn(*s, dtype=torch.bfloat16) * 3.2)(B, Hkv, Sk, Dh)),
            "wq": _w((lambda *s: torch.randn(*s, dtype=torch.bfloat16) * 0.8)(D, Hq * Dh) * (D ** -0.5)),
            "wo": _w((lambda *s: torch.randn(*s, dtype=torch.bfloat16) * 3.2)(Hq * Dh, D) * ((Hq * Dh) ** -0.5)),
        }
    _A_in = _A_setup()

    def _B_setup():
        B, S, D, F = 2, 1, 576, 4608
        return {
            "x": _w((lambda *s: torch.randn(*s, dtype=torch.bfloat16) * 0.1)(B, S, D)),
            "w1": _w((lambda *s: torch.randn(*s, dtype=torch.bfloat16) * 0.8)(S, F) * (S ** -0.5)),
            "b1": _w((lambda *s: torch.randn(*s, dtype=torch.bfloat16) * 0.8)(F) * 0.02),
            "w2": _w((lambda *s: torch.randn(*s, dtype=torch.bfloat16) * 0.4)(F, S) * (F ** -0.5)),
            "b2": _w((lambda *s: torch.randn(*s, dtype=torch.bfloat16) * 0.4)(S) * 0.02),
        }
    _B_in = _B_setup()

    return {
        "A_x": _A_in["x"],
        "A_k_cache": _A_in["k_cache"],
        "A_v_cache": _A_in["v_cache"],
        "A_wq": _A_in["wq"],
        "A_wo": _A_in["wo"],
        "B_x": _B_in["x"],
        "B_w1": _B_in["w1"],
        "B_b1": _B_in["b1"],
        "B_w2": _B_in["w2"],
        "B_b2": _B_in["b2"],
    }

def reference(A_x, A_k_cache, A_v_cache, A_wq, A_wo, B_x, B_w1, B_b1, B_w2, B_b2):
    def _A_ref(x, k_cache, v_cache, wq, wo):
        B, Sq, D = x.shape
        Hq, Dh = 32, 128
        Hkv = k_cache.shape[1]
        rep = Hq // Hkv
        q = (lambda x, *d: x.permute(*d))((x @ wq).reshape(B, Sq, Hq, Dh), 0, 2, 1, 3)
        k = (lambda x, n, axis: torch.repeat_interleave(x, n, dim=axis))(k_cache, rep, axis=1)
        v = (lambda x, n, axis: torch.repeat_interleave(x, n, dim=axis))(v_cache, rep, axis=1)
        scores = torch.einsum("bhqd,bhkd->bhqk", q, k) * (Dh ** -0.5)
        p = _softmax(scores, axis=-1)
        o = torch.einsum("bhqk,bhkd->bhqd", p, v)
        return (lambda x, *d: x.permute(*d))(o, 0, 2, 1, 3).reshape(B, Sq, Hq * Dh) @ wo

    def _B_ref(x, w1, b1, w2, b2):
        B, S, D = x.shape
        h = _layernorm(x, axis=-1)
        u = torch.einsum("bsd,sf->bdf", h, w1) + b1
        u = _gelu(u)
        y = torch.einsum("bdf,fs->bds", u, w2) + b2
        return (lambda x, *d: x.permute(*d))(y, 0, 2, 1)

    x = A_x
    x = x + _A_ref(_layernorm(x, axis=-1), A_k_cache, A_v_cache, A_wq, A_wo)
    x = x + _B_ref(_layernorm(x, axis=-1), B_w1, B_b1, B_w2, B_b2)
    x = x + _A_ref(_layernorm(x, axis=-1), A_k_cache, A_v_cache, A_wq, A_wo)
    x = x + _B_ref(_layernorm(x, axis=-1), B_w1, B_b1, B_w2, B_b2)
    return x
```

```cpp
#include <hip/hip_runtime.h>
#include <hip/hip_bf16.h>
#include <cstdint>
typedef unsigned short u16;
typedef __attribute__((ext_vector_type(4))) unsigned u32x4;
constexpr int B=2, D=576, HQ=32, HKV=16, SK=16384, DH=128, F=4608, NS=8;
__device__ __forceinline__ float bf2f(u16 v){ return __uint_as_float(((unsigned)v)<<16); }
__device__ __forceinline__ float rbf(float f){ return __bfloat162float(__float2bfloat16(f)); }
__device__ float block_sum(float v, float* red){
  for(int o=32;o>0;o>>=1) v+=__shfl_xor(v,o);
  __syncthreads();
  if((threadIdx.x&63)==0) red[threadIdx.x>>6]=v;
  __syncthreads();
  float s=0; for(int i=0;i<(int)(blockDim.x>>6);i++) s+=red[i];
  return s;
}
__device__ void ln_inplace(float* sx, float* red){
  float p=0; for(int i=threadIdx.x;i<D;i+=blockDim.x) p+=sx[i];
  float m=block_sum(p,red)/D;
  p=0; for(int i=threadIdx.x;i<D;i+=blockDim.x){float d=sx[i]-m; p+=d*d;}
  float v=block_sum(p,red)/D;
  float r=1.0f/sqrtf(v+1e-5f);
  __syncthreads();
  for(int i=threadIdx.x;i<D;i+=blockDim.x) sx[i]=rbf((sx[i]-m)*r);
  __syncthreads();
}
__global__ void k_init(const u16* x, float* xf){ int i=blockIdx.x*256+threadIdx.x; if(i<B*D) xf[i]=bf2f(x[i]); }
__global__ void k_q(const float* xf, const u16* wq, float* q){
  __shared__ float sx[D]; __shared__ float red[4]; __shared__ float part[4][64];
  int b=blockIdx.y;
  for(int i=threadIdx.x;i<D;i+=256) sx[i]=xf[b*D+i];
  __syncthreads();
  ln_inplace(sx,red);
  int c=blockIdx.x*64+(threadIdx.x&63), g=threadIdx.x>>6;
  float a=0; for(int k=g;k<D;k+=4) a+=sx[k]*bf2f(wq[(size_t)k*4096+c]);
  part[g][threadIdx.x&63]=a; __syncthreads();
  if(g==0) q[b*4096+c]=rbf(part[0][threadIdx.x]+part[1][threadIdx.x]+part[2][threadIdx.x]+part[3][threadIdx.x]);
}
constexpr int NSP=32;
__global__ void k_attn(const float* q, const u16* K, const u16* V, float* pm, float* pl, float* po){
  int s=blockIdx.x, kvh=blockIdx.y, b=blockIdx.z;
  int lane=threadIdx.x&63, w=threadIdx.x>>6, r=lane>>4, c=lane&15;
  __shared__ float sm[4][2], sl[4][2], so[4][2][128];
  float qf[2][8];
  for(int g=0;g<2;g++) for(int j=0;j<8;j++) qf[g][j]=q[b*4096+(kvh*2+g)*128+c*8+j]*0.08838834764831845f*1.4426950408889634f;
  const int KPS=SK/NSP;
  size_t base=((size_t)(b*HKV+kvh)*SK + (size_t)s*KPS)*DH;
  float m[2]={-INFINITY,-INFINITY}, l[2]={0,0}, acc[2][8];
  for(int g=0;g<2;g++) for(int j=0;j<8;j++) acc[g][j]=0;
  for(int k0=w*16;k0<KPS;k0+=64){
    u32x4 kk[4], vv[4];
    for(int t=0;t<4;t++){ size_t o=base+(size_t)(k0+t*4+r)*DH+c*8; kk[t]=__builtin_nontemporal_load((const u32x4*)(K+o)); vv[t]=__builtin_nontemporal_load((const u32x4*)(V+o)); }
    for(int t=0;t<4;t++){
      for(int g=0;g<2;g++){
        float d=0;
        for(int j=0;j<4;j++){ d+=__uint_as_float(kk[t][j]<<16)*qf[g][2*j]+__uint_as_float(kk[t][j]&0xffff0000u)*qf[g][2*j+1]; }
        for(int o=1;o<16;o<<=1) d+=__shfl_xor(d,o);
        float mn=fmaxf(m[g],d), al=exp2f(m[g]-mn), p=exp2f(d-mn);
        l[g]=l[g]*al+p; m[g]=mn;
        for(int j=0;j<4;j++){ acc[g][2*j]=acc[g][2*j]*al+p*__uint_as_float(vv[t][j]<<16); acc[g][2*j+1]=acc[g][2*j+1]*al+p*__uint_as_float(vv[t][j]&0xffff0000u);}
      }
    }
  }
  for(int g=0;g<2;g++){
    float M=m[g]; M=fmaxf(M,__shfl_xor(M,16)); M=fmaxf(M,__shfl_xor(M,32));
    float sc=exp2f(m[g]-M); float L=l[g]*sc; L+=__shfl_xor(L,16); L+=__shfl_xor(L,32);
    for(int j=0;j<8;j++){ float a=acc[g][j]*sc; a+=__shfl_xor(a,16); a+=__shfl_xor(a,32); if(r==0) so[w][g][c*8+j]=a; }
    if(lane==0){ sm[w][g]=M; sl[w][g]=L; }
  }
  __syncthreads();
  int g=threadIdx.x>>7, d=threadIdx.x&127;
  float M=fmaxf(fmaxf(sm[0][g],sm[1][g]),fmaxf(sm[2][g],sm[3][g]));
  float L=0,o=0; for(int i=0;i<4;i++){ float sc=exp2f(sm[i][g]-M); L+=sl[i][g]*sc; o+=so[i][g][d]*sc; }
  int idx=(b*HQ+kvh*2+g)*NSP+s;
  po[(size_t)idx*128+d]=o; if(d==0){pm[idx]=M; pl[idx]=L;}
}
__global__ void k_comb(const float* pm,const float* pl,const float* po,float* o){
  int h=blockIdx.x, d=threadIdx.x;
  float M=-INFINITY; for(int s=0;s<NSP;s++) M=fmaxf(M,pm[h*NSP+s]);
  float L=0,a=0; for(int s=0;s<NSP;s++){ float sc=exp2f(pm[h*NSP+s]-M); L+=pl[h*NSP+s]*sc; a+=po[(size_t)(h*NSP+s)*128+d]*sc; }
  o[h*128+d]=rbf(a/L);
}
__global__ void k_wo(const float* o,const u16* wo,float* xf){
  __shared__ float part[16][64];
  int b=blockIdx.y, c=blockIdx.x*64+(threadIdx.x&63), g=threadIdx.x>>6;
  float a=0; for(int k=g;k<4096;k+=16) a+=o[b*4096+k]*bf2f(wo[(size_t)k*D+c]);
  part[g][threadIdx.x&63]=a; __syncthreads();
  if(g==0){ float s=0; for(int i=0;i<16;i++) s+=part[i][threadIdx.x]; xf[b*D+c]=rbf(xf[b*D+c]+rbf(s)); }
}
__global__ void k_mix(const float* xin,float* xout,const u16* w1,const u16* b1,const u16* w2,const u16* b2,__hip_bfloat16* out){
  __shared__ float sx[D]; __shared__ float red[4];
  int b=blockIdx.y, d=blockIdx.x;
  for(int i=threadIdx.x;i<D;i+=256) sx[i]=xin[b*D+i];
  __syncthreads();
  ln_inplace(sx,red); ln_inplace(sx,red);
  float h=sx[d], a=0;
  for(int f=threadIdx.x;f<F;f+=256){ float u=h*bf2f(w1[f])+bf2f(b1[f]); float g=0.5f*u*(1.f+tanhf(0.7978845608028654f*(u+0.044715f*u*u*u))); a+=g*bf2f(w2[f]); }
  float s=block_sum(a,red);
  if(threadIdx.x==0){ float y=rbf(xin[b*D+d]+rbf(rbf(s)+bf2f(b2[0]))); xout[b*D+d]=y; if(out) out[b*D+d]=__float2bfloat16(y); }
}
extern "C" void kernel_launch(void* const* d_in, const int* in_sizes, int n_in, void* d_out, int out_size, void* d_ws, size_t ws_size, hipStream_t stream){
  const u16* x=(const u16*)d_in[0]; const u16* K=(const u16*)d_in[1]; const u16* V=(const u16*)d_in[2];
  const u16* wq=(const u16*)d_in[3]; const u16* wo=(const u16*)d_in[4];
  const u16* w1=(const u16*)d_in[6]; const u16* b1=(const u16*)d_in[7]; const u16* w2=(const u16*)d_in[8]; const u16* b2=(const u16*)d_in[9];
  float* ws=(float*)d_ws; float* xa=ws; float* xb=ws+2048; float* q=ws+4096; float* o=q+8192; float* pm=o+8192; float* pl=pm+4096; float* po=pl+4096;
  k_init<<<5,256,0,stream>>>(x,xa);
  for(int it=0;it<2;it++){
    k_q<<<dim3(64,2),256,0,stream>>>(xa,wq,q);
    k_attn<<<dim3(NSP,HKV,B),256,0,stream>>>(q,K,V,pm,pl,po);
    k_comb<<<64,128,0,stream>>>(pm,pl,po,o);
    k_wo<<<dim3(9,2),1024,0,stream>>>(o,wo,xa);
    k_mix<<<dim3(576,2),256,0,stream>>>(xa,xb,w1,b1,w2,b2,it==1?(__hip_bfloat16*)d_out:nullptr);
    float* t=xa; xa=xb; xb=t;
  }
}
```

```cpp
#include <hip/hip_runtime.h>
#pragma clang diagnostic ignored "-Wint-to-pointer-cast"
#pragma clang diagnostic ignored "-Winline-asm"
typedef unsigned u32; typedef unsigned long long u64; typedef unsigned short u16;
typedef __attribute__((ext_vector_type(4))) u32 u32x4;
typedef __attribute__((ext_vector_type(2))) u32 u32x2;
typedef __attribute__((ext_vector_type(4))) float f32x4;
typedef __attribute__((ext_vector_type(2))) float f32x2;
typedef __attribute__((ext_vector_type(2))) __bf16 bf16x2;
typedef __attribute__((ext_vector_type(8))) __bf16 bf16x8;
#define DEVI __device__ __forceinline__
#define LDSP(T, off) ((__attribute__((address_space(3))) T*)(off))
#define BAR() asm volatile("s_waitcnt lgkmcnt(0)\n\ts_barrier" ::: "memory")
#define F4(x) __builtin_bit_cast(f32x4, x)
constexpr u32 ROT = 1u;
constexpr u32 SSH = 14u;
constexpr u32 SEG = 2u;
constexpr u32 ONES = 16000u;
constexpr u32 ZC = 15968u;
constexpr u32 TAB = 0u;
constexpr u32 QB0 = 8192u;
constexpr u32 QB1 = 8704u;
constexpr u32 XR = 9216u;
constexpr u32 MRG = 11520u;
constexpr u32 MLB = 15616u;
constexpr u32 RED = 15680u;
constexpr u32 QRED = 11520u;
constexpr u32 EW = 16256u;
constexpr u32 STM = 16272u;
constexpr u32 W_Q1 = 1048576u;
constexpr u32 W_TAB = 36864u;
constexpr u32 W_P = 65536u;
constexpr u32 W_Y = 720896u;
constexpr u32 W_Q = 802816u;
constexpr u32 W_DBG = 16777216u;
constexpr u32 P_SZ = 163840u;
constexpr u32 Y_SZ = 20480u;
constexpr u32 Q_SZ = 65536u;
constexpr int SPIN = 1 << 16;

DEVI u32 LANE() { u32 l; asm volatile("v_mbcnt_lo_u32_b32 %0, -1, 0\n\tv_mbcnt_hi_u32_b32 %0, -1, %0" : "=v"(l)); return l; }
DEVI u64 pin(u64 x) { asm volatile("" : "+s"(x)); return x; }
DEVI u32 now32() { return (u32)__builtin_amdgcn_s_memrealtime(); }
DEVI void pace(u32 tb, u32 d) { while (now32() - tb < d) __builtin_amdgcn_s_sleep(1); }
DEVI void delay(u32 d) { pace(now32(), d); }
DEVI float lo16(u32 v) { return __uint_as_float(v << 16); }
DEVI float hi16(u32 v) { return __uint_as_float(v & 0xffff0000u); }
DEVI u32 pk2(float a, float b) { f32x2 v = {a, b}; return __builtin_bit_cast(u32, __builtin_convertvector(v, bf16x2)); }
DEVI float rbf(float a) { return lo16(pk2(a, a)); }
DEVI u32 umax3(u32 a, u32 b, u32 c) { return max(max(a, b), c); }
DEVI u32 umax4(u32x4 a) { return max(max(a[0], a[1]), max(a[2], a[3])); }
DEVI bool tagok(u32x4 a, u32 tag) { return a[1] == tag && a[3] == tag; }
DEVI u32 nz(u32 v) { return min(v, 0xFFFFFFFEu); }
template <int C> DEVI u32 dppu(u32 v) { return (u32)__builtin_amdgcn_update_dpp(0, (int)v, C, 0xf, 0xf, true); }
template <int C> DEVI float dppf(float v) { return __uint_as_float(dppu<C>(__float_as_uint(v))); }
DEVI float rowsum(float v) { v += dppf<0x128>(v); v += dppf<0x124>(v); v += dppf<0x122>(v); v += dppf<0x121>(v); return v; }
DEVI float rowmax(float v) { v = fmaxf(v, dppf<0x128>(v)); v = fmaxf(v, dppf<0x124>(v)); v = fmaxf(v, dppf<0x122>(v)); v = fmaxf(v, dppf<0x121>(v)); return v; }
DEVI float xrowsum(float x) {
  auto s = __builtin_amdgcn_permlane16_swap(__float_as_uint(x), __float_as_uint(x), false, false);
  x = __uint_as_float(s[0]) + __uint_as_float(s[1]);
  auto t = __builtin_amdgcn_permlane32_swap(__float_as_uint(x), __float_as_uint(x), false, false);
  return __uint_as_float(t[0]) + __uint_as_float(t[1]);
}
DEVI void st16(u32 off, u32x4 v, u64 base) { asm volatile("global_store_dwordx4 %0, %1, %2 sc1\n\ts_nop 1" :: "v"(off), "v"(v), "s"(base) : "memory"); }
DEVI void st4(u32 off, u32 v, u64 base) { asm volatile("global_store_dword %0, %1, %2 sc1" :: "v"(off), "v"(v), "s"(base) : "memory"); }
DEVI void st8(u32 off, u32x2 v, u64 base) { asm volatile("global_store_dwordx2 %0, %1, %2 sc1" :: "v"(off), "v"(v), "s"(base) : "memory"); }
DEVI void dma16(u32 off, u64 base, u32 lds) { asm volatile("s_mov_b32 m0, %2\n\ts_nop 0\n\tglobal_load_lds_dwordx4 %0, %1 nt" :: "v"(off), "s"(base), "s"(lds) : "memory"); }
DEVI void dma4(u32 off, u64 base, u32 lds) { asm volatile("s_mov_b32 m0, %2\n\ts_nop 0\n\tglobal_load_lds_dword %0, %1 nt" :: "v"(off), "s"(base), "s"(lds) : "memory"); }
DEVI void dma4c(u32 off, u64 base, u32 lds) { asm volatile("s_mov_b32 m0, %2\n\ts_nop 0\n\tglobal_load_lds_dword %0, %1 sc1" :: "v"(off), "s"(base), "s"(lds) : "memory"); }
constexpr float CSCALE = 0.08838834764831845f * 1.4426950408889634f;

DEVI f32x2 up2(u32 v) { return f32x2{lo16(v), hi16(v)}; }
DEVI u32 pkv(f32x2 v) { return __builtin_bit_cast(u32, __builtin_convertvector(v, bf16x2)); }
struct Cen { f32x2 d0, d1, d2, d3; float de, pv, md, var, s1, s2; };
DEVI Cen stats1(u32x4 p, u32 t) {
  Cen c;
  c.pv = __uint_as_float(__builtin_amdgcn_readfirstlane(p[0]) << 16);
  c.d0 = up2(p[0]) - c.pv; c.d1 = up2(p[1]) - c.pv; c.d2 = up2(p[2]) - c.pv; c.d3 = up2(p[3]) - c.pv;
  c.de = lo16(t) - c.pv;
  f32x2 s = (c.d0 + c.d1) + (c.d2 + c.d3), q = __builtin_elementwise_fma(c.d1, c.d1, c.d0 * c.d0) + __builtin_elementwise_fma(c.d3, c.d3, c.d2 * c.d2);
  c.s1 = (s[0] + c.de) + s[1]; c.s2 = fmaf(c.de, c.de, q[0]) + q[1];
  return c;
}
DEVI void stats2(Cen& c) {
  auto x = __builtin_amdgcn_permlane32_swap(__float_as_uint(c.s1), __float_as_uint(c.s2), false, false);
  float u = rowsum(__uint_as_float(x[0]) + __uint_as_float(x[1]));
  u = (u + dppf<0x142>(u)) * (1.f / 576.f);
  c.md = __uint_as_float(__builtin_amdgcn_readlane(__float_as_uint(u), 31));
  c.var = fmaxf(fmaf(-c.md, c.md, __uint_as_float(__builtin_amdgcn_readlane(__float_as_uint(u), 63))), 0.f);
}
DEVI void lnp(u32x4& p, u32& t) {
  Cen c = stats1(p, t); stats2(c);
  float r = __builtin_amdgcn_rsqf(c.var + 1e-5f);
  p[0] = pkv((c.d0 - c.md) * r); p[1] = pkv((c.d1 - c.md) * r); p[2] = pkv((c.d2 - c.md) * r); p[3] = pkv((c.d3 - c.md) * r);
  t = pk2((c.de - c.md) * r, 0.f);
}
DEVI u32x4 granules(float v, u32 tag) {
  u32 b = __float_as_uint(v);
  u32 p = (b >> 16) | dppu<0xB1>(b);
  u32 p2 = dppu<0x4E>(p);
  return u32x4{p, tag, p2, tag};
}
DEVI void table_publish(float (&acc)[16], int w, float b2f, u32 tag, u64 dst) {
  u32 ln = LANE();
#pragma unroll
  for (int e = 0; e < 8; e++) {
    auto s = __builtin_amdgcn_permlane32_swap(__float_as_uint(acc[e]), __float_as_uint(acc[e + 8]), false, false);
    acc[e] = __uint_as_float(s[0]) + __uint_as_float(s[1]);
  }
  f32x4 r;
#pragma unroll
  for (int e = 0; e < 4; e++) {
    auto s = __builtin_amdgcn_permlane16_swap(__float_as_uint(acc[e]), __float_as_uint(acc[e + 4]), false, false);
    r[e] = rowsum(__uint_as_float(s[0]) + __uint_as_float(s[1]));
  }
  if ((ln & 15u) == 0u) *LDSP(f32x4, QRED + w * 64u + ln) = r;
  BAR();
  if (w == 0) {
    u32 a = QRED + (ln & 15u) * 4u;
    float y = rbf(rbf((*LDSP(float, a) + *LDSP(float, a + 64)) + (*LDSP(float, a + 128) + *LDSP(float, a + 192))) + b2f);
    u32x4 g = granules(y, tag);
    if (ln < 16u && (ln & 3u) == 0u) st16(ln * 4u, g, dst);
  }
}
DEVI u32 tabof(float v) { return __builtin_amdgcn_ubfe(pk2(v, v), 14u, 16u); }
DEVI void mixer(u32 xr, u32 ln, u32x4& xp, u32& xt) {
  u32x4 p = *LDSP(u32x4, xr + ln * 16u); u32 t = *LDSP(u16, xr + 1024u + ln * 2u);
  f32x2 y0 = {0.f, 0.f}, y1 = y0, y2 = y0, y3 = y0; float y4 = 0.f;
  asm volatile("" : "+v"(y0), "+v"(y1), "+v"(y2), "+v"(y3), "+v"(y4));
  Cen c = stats1(p, t); stats2(c);
  const float A = 0x1p-58f;
  float r = __builtin_amdgcn_rsqf(fmaf(c.var, 1.00001f, 1e-10f)) * A, mo = -c.md * A;
  f32x2 A2 = {A, A}, m2 = {mo, mo};
  f32x2 h0 = __builtin_elementwise_fma(c.d0, A2, m2) * r, h1 = __builtin_elementwise_fma(c.d1, A2, m2) * r,
        h2 = __builtin_elementwise_fma(c.d2, A2, m2) * r, h3 = __builtin_elementwise_fma(c.d3, A2, m2) * r;
  float h4 = fmaf(c.de, A, mo) * r;
  asm volatile("ds_read_u16_d16_hi %0, %9\n\tds_read_u16_d16_hi %1, %10\n\tds_read_u16_d16_hi %2, %11\n\tds_read_u16_d16_hi %3, %12\n\tds_read_u16_d16_hi %4, %13\n\t"
               "ds_read_u16_d16_hi %5, %14\n\tds_read_u16_d16_hi %6, %15\n\tds_read_u16_d16_hi %7, %16\n\tds_read_u16_d16_hi %8, %17\n\ts_waitcnt lgkmcnt(0)"
               : "+v"(y0[0]), "+v"(y0[1]), "+v"(y1[0]), "+v"(y1[1]), "+v"(y2[0]), "+v"(y2[1]), "+v"(y3[0]), "+v"(y3[1]), "+v"(y4)
               : "v"(tabof(h0[0])), "v"(tabof(h0[1])), "v"(tabof(h1[0])), "v"(tabof(h1[1])), "v"(tabof(h2[0])), "v"(tabof(h2[1])), "v"(tabof(h3[0])), "v"(tabof(h3[1])), "v"(tabof(h4)) : "memory");
  xp[0] = pkv(up2(p[0]) + y0); xp[1] = pkv(up2(p[1]) + y1); xp[2] = pkv(up2(p[2]) + y2); xp[3] = pkv(up2(p[3]) + y3);
  xt = pk2(lo16(t) + y4, 0.f);
}
DEVI u32x4 ld8x2(u32 a) { u32x2 x = *LDSP(u32x2, a), y = *LDSP(u32x2, a + 8); return u32x4{x[0], x[1], y[0], y[1]}; }
DEVI u32 qpos(int w, u32 ln) { return (w >> 1) * 256u + (w & 1) * 16u + (ln >> 3) * 32u + (ln & 7u) * 2u; }
DEVI u32x4 frag16(u32 a, u32 stride) {
  u32x4 f;
#pragma unroll
  for (int j = 0; j < 4; j++) f[j] = (u32)*LDSP(u16, a + (2 * j) * stride) | ((u32)*LDSP(u16, a + (2 * j + 1) * stride) << 16);
  return f;
}
DEVI void q1_ln() {
  u32 ln = LANE();
#pragma unroll
  for (int b = 0; b < 2; b++) {
    u32x4 p = *LDSP(u32x4, XR + b * 1152u + ln * 16u); u32 t = *LDSP(u16, XR + b * 1152u + 1024u + ln * 2u);
    lnp(p, t);
    *LDSP(u32x4, MRG + b * 1152u + ln * 16u) = p;
    *LDSP(u16, MRG + b * 1152u + 1024u + ln * 2u) = (u16)t;
  }
}
DEVI void q1_poll(u32x4 a0, u32x4 a1, u32x4 a2, u32x4 a3, int w, u32 tag, u64 src) {
  u32 ln = LANE(); u32 o = ln * 16u;
#pragma unroll 1
  for (int sp = 0; sp < SPIN; sp++) {
    if (__all(tagok(a0, tag) && tagok(a1, tag) && tagok(a2, tag) && tagok(a3, tag))) break;
    __builtin_amdgcn_s_sleep(1);
    asm volatile("global_load_dwordx4 %0, %4, %5 sc1\n\tglobal_load_dwordx4 %1, %4, %5 offset:1024 sc1\n\tglobal_load_dwordx4 %2, %4, %5 offset:2048 sc1\n\tglobal_load_dwordx4 %3, %4, %5 offset:3072 sc1\n\ts_waitcnt vmcnt(0)"
                 : "=&v"(a0), "=&v"(a1), "=&v"(a2), "=&v"(a3) : "v"(o), "s"(src) : "memory");
  }
  f32x4 v = (F4(a0) + F4(a1)) + (F4(a2) + F4(a3));
  u32 r = pk2(v[0], v[2]), a = qpos(w, ln);
  *LDSP(u16, QB1 + a) = (u16)r; *LDSP(u16, QB0 + a) = (u16)(r >> 16);
  if (ln < 8u) *LDSP(u32, ZC + ln * 4u) = 0u;
  if (ln < 18u) *LDSP(u32, ONES + ln * 4u) = 0x3F803F80u;
}
DEVI u32 gpos(u32 c, u32 s) { return (c >> 2) * 1024u + ((c & 3u) / SEG) * (256u * SEG) + s * (16u * SEG) + (c % SEG) * 16u; }
DEVI void clear_slots(int w, u64 wsb, int lid, int xg, int i32, int kvh, int s) {
  u32 ln = LANE();
  u32 o = W_P + w * P_SZ + kvh * 10240u + (ln < 32u ? gpos(ln, s) : 8192u + s * 16u);
  if (ln >= 33u) o = W_Y + w * Y_SZ + xg * 2560u + i32 * 80u + (ln - 33u) * 16u;
  if (ln < 38u) st16(o, u32x4{~0u, ~0u, ~0u, ~0u}, wsb);
  u64 qd = pin(wsb + (u64)(W_Q + kvh * 4096u + (s & 3) * 1024u + w * 256u + (s >> 2) * 4u));
  if (ln < 32u) st4((ln >> 4) * Q_SZ + (ln & 15u) * 16u, ~0u, qd);
}
DEVI u32 merge_publish(int w, int s, u64 dst) {
  u32 ln = LANE(), go = gpos(ln >> 1, s), mo = 8192u + s * 16u + (ln >> 2), a0 = (ln >> 5) * 32u, a1 = ln * 16u;
  dst = pin(dst); asm volatile("" : "+v"(go), "+v"(mo), "+v"(a0), "+v"(a1));
  BAR();
  u32 tr = now32();
  if (w == 0) {
    f32x4 p = *LDSP(f32x4, MLB + a0), q = *LDSP(f32x4, MLB + a0 + 16u);
    f32x4 v0 = *LDSP(f32x4, MRG + a1), v1 = *LDSP(f32x4, MRG + 1024u + a1), v2 = *LDSP(f32x4, MRG + 2048u + a1), v3 = *LDSP(f32x4, MRG + 3072u + a1);
    float Mx = fmaxf(fmaxf(p[0], p[2]), fmaxf(q[0], q[2]));
    float e0 = __builtin_amdgcn_exp2f(p[0] - Mx), e1 = __builtin_amdgcn_exp2f(p[2] - Mx), e2 = __builtin_amdgcn_exp2f(q[0] - Mx), e3 = __builtin_amdgcn_exp2f(q[2] - Mx);
    float L = (e0 * p[1] + e1 * p[3]) + (e2 * q[1] + e3 * q[3]);
    float r = __builtin_amdgcn_rcpf(L);
    f32x4 o = ((v0 * e0 + v1 * e1) + (v2 * e2 + v3 * e3)) * r;
    u32 ga = nz(pk2(o[0], o[1])), gb = nz(pk2(o[2], o[3]));
    u32x4 g = {ga, gb, dppu<0xB1>(ga), dppu<0xB1>(gb)};
    if ((ln & 1u) == 0u) st16(go, g, dst);
    if ((ln & 31u) == 0u) st8(mo, u32x2{nz(__float_as_uint(Mx)), nz(__float_as_uint(L))}, dst);
  }
  return tr;
}
DEVI void wo_finish(u32 ln, int w, u64 dst) {
  if (w == 0) {
    f32x4 v = *LDSP(f32x4, RED + min(ln, 17u) * 16u);
    u32 y = nz(__float_as_uint((v[0] + v[1]) + (v[2] + v[3])));
    u32x4 g = {y, dppu<0x55>(y), dppu<0xAA>(y), dppu<0xFF>(y)};
    if (ln < 20u && (ln & 3u) == 0u) st16(ln * 4u, g, dst);
  }
}

__global__ __launch_bounds__(256, 1) __attribute__((amdgpu_num_vgpr(44)))
void fused_kernel(const char* xin, const char* Kp, const char* Vp, const char* wq, const char* wo,
                  const u32* w1p, const u32* b1p, const u32* w2p, const char* b2p, char* outp, char* ws) {
  asm volatile("" ::: "v44", "v255", "a0", "a255");
  const u32 t0 = now32();
  u32 tb = t0;
  const int wg = blockIdx.x, xg = wg & 7, i32 = wg >> 3, kvh = 2 * xg + (i32 & 1), s = i32 >> 1, lid = kvh * 16 + s;
  const int w = __builtin_amdgcn_readfirstlane(threadIdx.x >> 6);
  const u32 lb = 16384u + w * 36864u;
  const u64 wsb = (u64)ws;
  const u64 kvo = ((u64)kvh << 22) + ((u64)s << SSH) + (u64)(w * 4096);
  const u64 Kb = (u64)Kp + kvo, Vb = (u64)Vp + kvo;
  const float CSC = CSCALE;
  const u64 wqb = (u64)wq + (u64)(((s >> 2) * 144 + w * 36) * 8192 + kvh * 512 + (s & 3) * 128), wob = (u64)wo + (u64)((xg * 512 + i32 * 16 + w * 4) * 1152),
            wog = (u64)wo + (u64)((4 * xg + w) * 147456 + i32 * 36);
  u64 cbase; u32 clen;
  asm volatile("s_getpc_b64 %0\n.Lcode_begin:\n\ts_mov_b32 %1, .Lcode_end-.Lcode_begin" : "=s"(cbase), "=s"(clen));
  {
    u32 ln = threadIdx.x & 63u, rt = (ROT == 1u ? kvh : ROT == 2u ? (kvh + s) & 15 : ROT == 3u ? (kvh * 4 + (kvh >> 2)) & 15 : 0) << 18;
    asm volatile("v_mov_b32 v44, %0\n\tv_mov_b32 v45, %1\n\tv_mov_b32 v46, %2\n\tv_mov_b32 v47, %3"
                 :: "v"(rt + (ln & 12u) * 256u + (ln & 2u) * 128u + (ln >> 4) * 32u + (ln & 1u) * 16u), "v"(rt + (ln >> 4) * 1024u + (ln & 15u) * 16u), "v"(lb + ln * 16u), "v"((ln & 4u) * 64u + (ln >> 3) * 32u));
    asm volatile("v_mov_b32 v52, %0" :: "v"((ln / 9u) * 1152u + (ln % 9u) * 4u));
    u32 sv = (ln & 1u) ? 0xFFFF0000u : 0x0000FFFFu, d = ((ln >> 1) & 1u) | ((ln >> 2) & 2u);
    asm volatile("v_mov_b32 v60, %0\n\tv_mov_b32 v61, %1\n\tv_mov_b32 v62, %2\n\tv_mov_b32 v63, %3"
                 :: "v"(d == 0u ? sv : 0u), "v"(d == 1u ? sv : 0u), "v"(d == 2u ? sv : 0u), "v"(d == 3u ? sv : 0u));
  }
#define CODE_PREFETCH(RA) do { u32 o_ = min((u32)(i32 * 4 + w) * 512u + (LANE() & 31u) * 16u, clen - 32u) & ~3u; \
    asm volatile("global_load_dwordx4 " RA ", %0, %1" :: "v"(o_), "s"(cbase) : "memory"); } while (0)
#define XDMA() do { u32 ln = LANE(); if (ln == 0u) { dma4c(0u, wsb, EW); dma4(0u, (u64)b2p, EW + 4u); } \
    if (ln < 36u) dma16(ln * 16u, (u64)xin + (u64)(w * 576), XR + w * 576u); } while (0)
#define WQDMA(i) do { u32 ln = LANE(); if (i < 4 || ln < 32u) \
    dma16((ln >> 3) * 8192u + (ln & 7u) * 16u, wqb + (u64)(i * 65536), lb + i * 1024u); } while (0)

  const u32 hb0 = (0x3A00u + (u32)lid * 8u) << 16;
  u32x4 xsave; u32 xsave_t, tref;
  { u32 o_ = w*2304u + LANE()*16u; asm volatile("global_load_dwordx4 a[176:179], %0, %1 offset:0\n\tglobal_load_dwordx4 a[180:183], %0, %2 offset:0\n\tglobal_load_dwordx4 a[184:187], %0, %3 offset:0" :  : "v"(o_), "s"(w1p), "s"(b1p), "s"(w2p) : "memory"); }
  XDMA();
  WQDMA(0);
  WQDMA(1);
  WQDMA(2);
  WQDMA(3);
  WQDMA(4);
  asm volatile("v_xor_b32 v43, 0x0, v44\n\tglobal_load_dwordx4 v[64:67], v43, %0 offset:0 nt\n\tglobal_load_dwordx4 v[68:71], v43, %0 offset:128 nt\n\tglobal_load_dwordx4 v[72:75], v43, %0 offset:512 nt\n\tglobal_load_dwordx4 v[76:79], v43, %0 offset:640 nt" :  : "s"(Kb) : "memory","scc","v43","v42","s98","s99");
  asm volatile("v_xor_b32 v43, 0x40000, v44\n\tglobal_load_dwordx4 v[80:83], v43, %0 offset:0 nt\n\tglobal_load_dwordx4 v[84:87], v43, %0 offset:128 nt\n\tglobal_load_dwordx4 v[88:91], v43, %0 offset:512 nt\n\tglobal_load_dwordx4 v[92:95], v43, %0 offset:640 nt" :  : "s"(Kb) : "memory","scc","v43","v42","s98","s99");
  CODE_PREFETCH("a[240:243]");
  asm volatile("s_waitcnt vmcnt(17)" :  :  : "memory");
  asm volatile("v_mov_b32 v240, 0\n\tv_mov_b32 v241, 0\n\tv_mov_b32 v242, 0\n\tv_mov_b32 v243, 0\n\tv_mov_b32 v244, 0\n\tv_mov_b32 v245, 0\n\tv_mov_b32 v246, 0\n\tv_mov_b32 v247, 0\n\tv_mov_b32 v248, 0\n\tv_mov_b32 v249, 0\n\tv_mov_b32 v250, 0\n\tv_mov_b32 v251, 0\n\tv_mov_b32 v252, 0\n\tv_mov_b32 v253, 0\n\tv_mov_b32 v254, 0\n\tv_mov_b32 v255, 0\n\tv_accvgpr_read_b32 v14, a176\n\tv_accvgpr_read_b32 v15, a180\n\tv_accvgpr_read_b32 v12, a184\n\ts_mov_b32 s84, %0\n\ts_call_b64 s[96:97], .Ltab\n\tv_xor_b32 v43, 0x80000, v44\n\tglobal_load_dwordx4 v[96:99], v43, %1 offset:0 nt\n\tglobal_load_dwordx4 v[100:103], v43, %1 offset:128 nt\n\tglobal_load_dwordx4 v[104:107], v43, %1 offset:512 nt\n\tglobal_load_dwordx4 v[108:111], v43, %1 offset:640 nt\n\ts_call_b64 s[96:97], .Ltab2\n\ts_branch .Ltabend\n\t.p2align 3\n\t.Ltab:\n\ts_mov_b32 s94, 0xffff0000\n\tv_lshlrev_b32 v8, 16, v14\n\tv_and_b32 v9, s94, v14\n\tv_lshlrev_b32 v10, 16, v15\n\tv_and_b32 v11, s94, v15\n\tv_and_b32 v13, s94, v12\n\tv_lshlrev_b32 v12, 16, v12\n\ts_cmp_lt_u32 s84, 0x3a800000\n\ts_cselect_b32 s95, 0x3a800000, 0\n\ts_or_b32 s84, s84, s95\n\tv_pk_fma_f32 v[40:41], s[94:95], v[8:9], v[10:11] op_sel:[1,0,0] op_sel_hi:[1,1,1]\n\tv_pk_fma_f32 v[10:11], s[94:95], v[8:9], v[10:11] op_sel:[1,0,0] op_sel_hi:[1,1,1] neg_lo:[1,0,0] neg_hi:[1,0,0]\n\ts_add_u32 s85, s84, 0x10000\n\ts_add_u32 s86, s84, 0x20000\n\ts_add_u32 s87, s84, 0x30000\n\ts_add_u32 s88, s84, 0x40000\n\ts_add_u32 s89, s84, 0x50000\n\ts_add_u32 s90, s84, 0x60000\n\ts_add_u32 s91, s84, 0x70000\n\ts_mov_b32 s92, 0x3dd2d3e8\n\ts_mov_b32 s93, 0x40135761\n\tv_pk_mul_f32 v[14:15], v[12:13], -0.5 op_sel_hi:[1,0]\n\tv_pk_fma_f32 v[16:17], s[84:85], v[8:9], v[10:11] op_sel_hi:[0,1,1]\n\tv_pk_fma_f32 v[18:19], s[84:85], v[8:9], v[40:41] op_sel_hi:[0,1,1] neg_lo:[1,0,0] neg_hi:[1,0,0]\n\tv_pk_mul_f32 v[20:21], v[16:17], v[16:17]\n\tv_pk_mul_f32 v[22:23], v[18:19], v[18:19]\n\tv_pk_fma_f32 v[20:21], v[20:21], s[92:93], s[92:93] op_sel:[0,0,1] op_sel_hi:[1,0,1]\n\tv_pk_fma_f32 v[22:23], v[22:23], s[92:93], s[92:93] op_sel:[0,0,1] op_sel_hi:[1,0,1]\n\tv_pk_mul_f32 v[20:21], v[20:21], v[16:17]\n\tv_pk_mul_f32 v[22:23], v[22:23], v[18:19]\n\tv_exp_f32_e64 v20, -abs(v20)\n\tv_exp_f32_e64 v21, -abs(v21)\n\tv_exp_f32_e64 v22, -abs(v22)\n\tv_exp_f32_e64 v23, -abs(v23)\n\tv_pk_fma_f32 v[28:29], s[84:85], v[8:9], v[10:11] op_sel:[1,0,0] op_sel_hi:[1,1,1]\n\tv_pk_add_f32 v[20:21], v[20:21], 1.0 op_sel_hi:[1,0]\n\tv_pk_fma_f32 v[30:31], s[84:85], v[8:9], v[40:41] op_sel:[1,0,0] op_sel_hi:[1,1,1] neg_lo:[1,0,0] neg_hi:[1,0,0]\n\tv_pk_add_f32 v[22:23], v[22:23], 1.0 op_sel_hi:[1,0]\n\tv_pk_mul_f32 v[32:33], v[28:29], v[28:29]\n\tv_pk_mul_f32 v[24:25], v[20:21], v[22:23]\n\tv_pk_mul_f32 v[34:35], v[30:31], v[30:31]\n\tv_pk_mul_f32 v[26:27], v[24:25], v[12:13] op_sel:[1,0] op_sel_hi:[0,1]\n\tv_pk_fma_f32 v[32:33], v[32:33], s[92:93], s[92:93] op_sel:[0,0,1] op_sel_hi:[1,0,1]\n\tv_mul_f32 v24, v24, v25\n\tv_pk_fma_f32 v[34:35], v[34:35], s[92:93], s[92:93] op_sel:[0,0,1] op_sel_hi:[1,0,1]\n\tv_rcp_f32 v24, v24\n\tv_pk_mul_f32 v[32:33], v[32:33], v[28:29]\n\tv_pk_mul_f32 v[26:27], v[26:27], v[24:25] op_sel_hi:[1,0]\n\tv_pk_mul_f32 v[34:35], v[34:35], v[30:31]\n\tv_pk_fma_f32 v[22:23], v[26:27], v[22:23], v[14:15]\n\tv_exp_f32_e64 v32, -abs(v32)\n\tv_pk_fma_f32 v[20:21], v[26:27], v[20:21], v[14:15]\n\tv_exp_f32_e64 v33, -abs(v33)\n\tv_fma_f32 v240, abs(v16), v22, v240\n\tv_exp_f32_e64 v34, -abs(v34)\n\tv_fma_f32 v241, abs(v18), v20, v241\n\tv_exp_f32_e64 v35, -abs(v35)\n\tv_fma_f32 v240, abs(v17), v23, v240\n\tv_fma_f32 v241, abs(v19), v21, v241\n\tv_pk_fma_f32 v[16:17], s[86:87], v[8:9], v[10:11] op_sel_hi:[0,1,1]\n\tv_pk_add_f32 v[32:33], v[32:33], 1.0 op_sel_hi:[1,0]\n\tv_pk_fma_f32 v[18:19], s[86:87], v[8:9], v[40:41] op_sel_hi:[0,1,1] neg_lo:[1,0,0] neg_hi:[1,0,0]\n\tv_pk_add_f32 v[34:35], v[34:35], 1.0 op_sel_hi:[1,0]\n\tv_pk_mul_f32 v[20:21], v[16:17], v[16:17]\n\tv_pk_mul_f32 v[36:37], v[32:33], v[34:35]\n\tv_pk_mul_f32 v[22:23], v[18:19], v[18:19]\n\tv_pk_mul_f32 v[38:39], v[36:37], v[12:13] op_sel:[1,0] op_sel_hi:[0,1]\n\tv_pk_fma_f32 v[20:21], v[20:21], s[92:93], s[92:93] op_sel:[0,0,1] op_sel_hi:[1,0,1]\n\tv_mul_f32 v36, v36, v37\n\tv_pk_fma_f32 v[22:23], v[22:23], s[92:93], s[92:93] op_sel:[0,0,1] op_sel_hi:[1,0,1]\n\tv_rcp_f32 v36, v36\n\tv_pk_mul_f32 v[20:21], v[20:21], v[16:17]\n\tv_pk_mul_f32 v[38:39], v[38:39], v[36:37] op_sel_hi:[1,0]\n\tv_pk_mul_f32 v[22:23], v[22:23], v[18:19]\n\tv_pk_fma_f32 v[34:35], v[38:39], v[34:35], v[14:15]\n\tv_exp_f32_e64 v20, -abs(v20)\n\tv_pk_fma_f32 v[32:33], v[38:39], v[32:33], v[14:15]\n\tv_exp_f32_e64 v21, -abs(v21)\n\tv_fma_f32 v242, abs(v28), v34, v242\n\tv_exp_f32_e64 v22, -abs(v22)\n\tv_fma_f32 v243, abs(v30), v32, v243\n\tv_exp_f32_e64 v23, -abs(v23)\n\tv_fma_f32 v242, abs(v29), v35, v242\n\tv_fma_f32 v243, abs(v31), v33, v243\n\tv_pk_fma_f32 v[28:29], s[86:87], v[8:9], v[10:11] op_sel:[1,0,0] op_sel_hi:[1,1,1]\n\tv_pk_add_f32 v[20:21], v[20:21], 1.0 op_sel_hi:[1,0]\n\tv_pk_fma_f32 v[30:31], s[86:87], v[8:9], v[40:41] op_sel:[1,0,0] op_sel_hi:[1,1,1] neg_lo:[1,0,0] neg_hi:[1,0,0]\n\tv_pk_add_f32 v[22:23], v[22:23], 1.0 op_sel_hi:[1,0]\n\tv_pk_mul_f32 v[32:33], v[28:29], v[28:29]\n\tv_pk_mul_f32 v[24:25], v[20:21], v[22:23]\n\tv_pk_mul_f32 v[34:35], v[30:31], v[30:31]\n\tv_pk_mul_f32 v[26:27], v[24:25], v[12:13] op_sel:[1,0] op_sel_hi:[0,1]\n\tv_pk_fma_f32 v[32:33], v[32:33], s[92:93], s[92:93] op_sel:[0,0,1] op_sel_hi:[1,0,1]\n\tv_mul_f32 v24, v24, v25\n\tv_pk_fma_f32 v[34:35], v[34:35], s[92:93], s[92:93] op_sel:[0,0,1] op_sel_hi:[1,0,1]\n\tv_rcp_f32 v24, v24\n\tv_pk_mul_f32 v[32:33], v[32:33], v[28:29]\n\tv_pk_mul_f32 v[26:27], v[26:27], v[24:25] op_sel_hi:[1,0]\n\tv_pk_mul_f32 v[34:35], v[34:35], v[30:31]\n\tv_pk_fma_f32 v[22:23], v[26:27], v[22:23], v[14:15]\n\tv_exp_f32_e64 v32, -abs(v32)\n\tv_pk_fma_f32 v[20:21], v[26:27], v[20:21], v[14:15]\n\tv_exp_f32_e64 v33, -abs(v33)\n\tv_fma_f32 v244, abs(v16), v22, v244\n\tv_exp_f32_e64 v34, -abs(v34)\n\tv_fma_f32 v245, abs(v18), v20, v245\n\tv_exp_f32_e64 v35, -abs(v35)\n\tv_fma_f32 v244, abs(v17), v23, v244\n\tv_fma_f32 v245, abs(v19), v21, v245\n\ts_setpc_b64 s[96:97]\n\t.Ltab2:\n\tv_pk_fma_f32 v[16:17], s[88:89], v[8:9], v[10:11] op_sel_hi:[0,1,1]\n\tv_pk_add_f32 v[32:33], v[32:33], 1.0 op_sel_hi:[1,0]\n\tv_pk_fma_f32 v[18:19], s[88:89], v[8:9], v[40:41] op_sel_hi:[0,1,1] neg_lo:[1,0,0] neg_hi:[1,0,0]\n\tv_pk_add_f32 v[34:35], v[34:35], 1.0 op_sel_hi:[1,0]\n\tv_pk_mul_f32 v[20:21], v[16:17], v[16:17]\n\tv_pk_mul_f32 v[36:37], v[32:33], v[34:35]\n\tv_pk_mul_f32 v[22:23], v[18:19], v[18:19]\n\tv_pk_mul_f32 v[38:39], v[36:37], v[12:13] op_sel:[1,0] op_sel_hi:[0,1]\n\tv_pk_fma_f32 v[20:21], v[20:21], s[92:93], s[92:93] op_sel:[0,0,1] op_sel_hi:[1,0,1]\n\tv_mul_f32 v36, v36, v37\n\tv_pk_fma_f32 v[22:23], v[22:23], s[92:93], s[92:93] op_sel:[0,0,1] op_sel_hi:[1,0,1]\n\tv_rcp_f32 v36, v36\n\tv_pk_mul_f32 v[20:21], v[20:21], v[16:17]\n\tv_pk_mul_f32 v[38:39], v[38:39], v[36:37] op_sel_hi:[1,0]\n\tv_pk_mul_f32 v[22:23], v[22:23], v[18:19]\n\tv_pk_fma_f32 v[34:35], v[38:39], v[34:35], v[14:15]\n\tv_exp_f32_e64 v20, -abs(v20)\n\tv_pk_fma_f32 v[32:33], v[38:39], v[32:33], v[14:15]\n\tv_exp_f32_e64 v21, -abs(v21)\n\tv_fma_f32 v246, abs(v28), v34, v246\n\tv_exp_f32_e64 v22, -abs(v22)\n\tv_fma_f32 v247, abs(v30), v32, v247\n\tv_exp_f32_e64 v23, -abs(v23)\n\tv_fma_f32 v246, abs(v29), v35, v246\n\tv_fma_f32 v247, abs(v31), v33, v247\n\tv_pk_fma_f32 v[28:29], s[88:89], v[8:9], v[10:11] op_sel:[1,0,0] op_sel_hi:[1,1,1]\n\tv_pk_add_f32 v[20:21], v[20:21], 1.0 op_sel_hi:[1,0]\n\tv_pk_fma_f32 v[30:31], s[88:89], v[8:9], v[40:41] op_sel:[1,0,0] op_sel_hi:[1,1,1] neg_lo:[1,0,0] neg_hi:[1,0,0]\n\tv_pk_add_f32 v[22:23], v[22:23], 1.0 op_sel_hi:[1,0]\n\tv_pk_mul_f32 v[32:33], v[28:29], v[28:29]\n\tv_pk_mul_f32 v[24:25], v[20:21], v[22:23]\n\tv_pk_mul_f32 v[34:35], v[30:31], v[30:31]\n\tv_pk_mul_f32 v[26:27], v[24:25], v[12:13] op_sel:[1,0] op_sel_hi:[0,1]\n\tv_pk_fma_f32 v[32:33], v[32:33], s[92:93], s[92:93] op_sel:[0,0,1] op_sel_hi:[1,0,1]\n\tv_mul_f32 v24, v24, v25\n\tv_pk_fma_f32 v[34:35], v[34:35], s[92:93], s[92:93] op_sel:[0,0,1] op_sel_hi:[1,0,1]\n\tv_rcp_f32 v24, v24\n\tv_pk_mul_f32 v[32:33], v[32:33], v[28:29]\n\tv_pk_mul_f32 v[26:27], v[26:27], v[24:25] op_sel_hi:[1,0]\n\tv_pk_mul_f32 v[34:35], v[34:35], v[30:31]\n\tv_pk_fma_f32 v[22:23], v[26:27], v[22:23], v[14:15]\n\tv_exp_f32_e64 v32, -abs(v32)\n\tv_pk_fma_f32 v[20:21], v[26:27], v[20:21], v[14:15]\n\tv_exp_f32_e64 v33, -abs(v33)\n\tv_fma_f32 v248, abs(v16), v22, v248\n\tv_exp_f32_e64 v34, -abs(v34)\n\tv_fma_f32 v249, abs(v18), v20, v249\n\tv_exp_f32_e64 v35, -abs(v35)\n\tv_fma_f32 v248, abs(v17), v23, v248\n\tv_fma_f32 v249, abs(v19), v21, v249\n\tv_pk_fma_f32 v[16:17], s[90:91], v[8:9], v[10:11] op_sel_hi:[0,1,1]\n\tv_pk_add_f32 v[32:33], v[32:33], 1.0 op_sel_hi:[1,0]\n\tv_pk_fma_f32 v[18:19], s[90:91], v[8:9], v[40:41] op_sel_hi:[0,1,1] neg_lo:[1,0,0] neg_hi:[1,0,0]\n\tv_pk_add_f32 v[34:35], v[34:35], 1.0 op_sel_hi:[1,0]\n\tv_pk_mul_f32 v[20:21], v[16:17], v[16:17]\n\tv_pk_mul_f32 v[36:37], v[32:33], v[34:35]\n\tv_pk_mul_f32 v[22:23], v[18:19], v[18:19]\n\tv_pk_mul_f32 v[38:39], v[36:37], v[12:13] op_sel:[1,0] op_sel_hi:[0,1]\n\tv_pk_fma_f32 v[20:21], v[20:21], s[92:93], s[92:93] op_sel:[0,0,1] op_sel_hi:[1,0,1]\n\tv_mul_f32 v36, v36, v37\n\tv_pk_fma_f32 v[22:23], v[22:23], s[92:93], s[92:93] op_sel:[0,0,1] op_sel_hi:[1,0,1]\n\tv_rcp_f32 v36, v36\n\tv_pk_mul_f32 v[20:21], v[20:21], v[16:17]\n\tv_pk_mul_f32 v[38:39], v[38:39], v[36:37] op_sel_hi:[1,0]\n\tv_pk_mul_f32 v[22:23], v[22:23], v[18:19]\n\tv_pk_fma_f32 v[34:35], v[38:39], v[34:35], v[14:15]\n\tv_exp_f32_e64 v20, -abs(v20)\n\tv_pk_fma_f32 v[32:33], v[38:39], v[32:33], v[14:15]\n\tv_exp_f32_e64 v21, -abs(v21)\n\tv_fma_f32 v250, abs(v28), v34, v250\n\tv_exp_f32_e64 v22, -abs(v22)\n\tv_fma_f32 v251, abs(v30), v32, v251\n\tv_exp_f32_e64 v23, -abs(v23)\n\tv_fma_f32 v250, abs(v29), v35, v250\n\tv_fma_f32 v251, abs(v31), v33, v251\n\tv_pk_fma_f32 v[28:29], s[90:91], v[8:9], v[10:11] op_sel:[1,0,0] op_sel_hi:[1,1,1]\n\tv_pk_add_f32 v[20:21], v[20:21], 1.0 op_sel_hi:[1,0]\n\tv_pk_fma_f32 v[30:31], s[90:91], v[8:9], v[40:41] op_sel:[1,0,0] op_sel_hi:[1,1,1] neg_lo:[1,0,0] neg_hi:[1,0,0]\n\tv_pk_add_f32 v[22:23], v[22:23], 1.0 op_sel_hi:[1,0]\n\tv_pk_mul_f32 v[32:33], v[28:29], v[28:29]\n\tv_pk_mul_f32 v[24:25], v[20:21], v[22:23]\n\tv_pk_mul_f32 v[34:35], v[30:31], v[30:31]\n\tv_pk_mul_f32 v[26:27], v[24:25], v[12:13] op_sel:[1,0] op_sel_hi:[0,1]\n\tv_pk_fma_f32 v[32:33], v[32:33], s[92:93], s[92:93] op_sel:[0,0,1] op_sel_hi:[1,0,1]\n\tv_mul_f32 v24, v24, v25\n\tv_pk_fma_f32 v[34:35], v[34:35], s[92:93], s[92:93] op_sel:[0,0,1] op_sel_hi:[1,0,1]\n\tv_rcp_f32 v24, v24\n\tv_pk_mul_f32 v[32:33], v[32:33], v[28:29]\n\tv_pk_mul_f32 v[26:27], v[26:27], v[24:25] op_sel_hi:[1,0]\n\tv_pk_mul_f32 v[34:35], v[34:35], v[30:31]\n\tv_pk_fma_f32 v[22:23], v[26:27], v[22:23], v[14:15]\n\tv_exp_f32_e64 v32, -abs(v32)\n\tv_pk_fma_f32 v[20:21], v[26:27], v[20:21], v[14:15]\n\tv_exp_f32_e64 v33, -abs(v33)\n\tv_fma_f32 v252, abs(v16), v22, v252\n\tv_exp_f32_e64 v34, -abs(v34)\n\tv_fma_f32 v253, abs(v18), v20, v253\n\tv_exp_f32_e64 v35, -abs(v35)\n\tv_fma_f32 v252, abs(v17), v23, v252\n\tv_fma_f32 v253, abs(v19), v21, v253\n\ts_nop 0\n\tv_pk_add_f32 v[32:33], v[32:33], 1.0 op_sel_hi:[1,0]\n\tv_pk_add_f32 v[34:35], v[34:35], 1.0 op_sel_hi:[1,0]\n\tv_pk_mul_f32 v[36:37], v[32:33], v[34:35]\n\tv_pk_mul_f32 v[38:39], v[36:37], v[12:13] op_sel:[1,0] op_sel_hi:[0,1]\n\tv_mul_f32 v36, v36, v37\n\tv_rcp_f32 v36, v36\n\ts_nop 0\n\tv_pk_mul_f32 v[38:39], v[38:39], v[36:37] op_sel_hi:[1,0]\n\tv_pk_fma_f32 v[34:35], v[38:39], v[34:35], v[14:15]\n\tv_pk_fma_f32 v[32:33], v[38:39], v[32:33], v[14:15]\n\tv_fma_f32 v254, abs(v28), v34, v254\n\tv_fma_f32 v255, abs(v30), v32, v255\n\tv_fma_f32 v254, abs(v29), v35, v254\n\tv_fma_f32 v255, abs(v31), v33, v255\n\ts_setpc_b64 s[96:97]\n\t.Ltabend:\n\tv_xor_b32 v43, 0xc0000, v44\n\tglobal_load_dwordx4 v[112:115], v43, %1 offset:0 nt\n\tglobal_load_dwordx4 v[116:119], v43, %1 offset:128 nt\n\tglobal_load_dwordx4 v[120:123], v43, %1 offset:512 nt\n\tglobal_load_dwordx4 v[124:127], v43, %1 offset:640 nt" :  : "s"(hb0), "s"(Kb) : "memory","scc","v8","v9","v10","v11","v12","v13","v14","v15","v16","v17","v18","v19","v20","v21","v22","v23","v24","v25","v26","v27","v28","v29","v30","v31","v32","v33","v34","v35","v36","v37","v38","v39","v40","v41","v42","v43","s84","s85","s86","s87","s88","s89","s90","s91","s92","s93","s94","s95","s96","s97","s98","s99");
  { u32 ln = LANE(); asm volatile("global_load_dwordx4 a[208:211], %0, %1 offset:-2048\n\tglobal_load_dwordx4 a[212:215], %0, %1 offset:-1024\n\tglobal_load_dwordx4 a[216:219], %0, %1 offset:0\n\tglobal_load_dwordx4 a[220:223], %0, %1 offset:1024\n\tglobal_load_dwordx2 v[54:55], %2, %1 offset:2048" :  : "v"(ln*16u), "s"(wob + 2048ull), "v"(ln*8u) : "memory"); }
  asm volatile("s_waitcnt vmcnt(22)\n\ts_barrier" :  :  : "memory");
  const u32 tag = __builtin_amdgcn_readfirstlane(*LDSP(u32, 16256u)) + 1u; const float b2f = lo16(__builtin_amdgcn_readfirstlane(*LDSP(u32, 16260u)));
  q1_ln();
  asm volatile("v_xor_b32 v43, 0x0, v45\n\tglobal_load_dwordx4 v[128:131], v43, %0 offset:0 nt\n\tglobal_load_dwordx4 v[132:135], v43, %0 offset:256 nt\n\tglobal_load_dwordx4 v[136:139], v43, %0 offset:512 nt\n\tglobal_load_dwordx4 v[140:143], v43, %0 offset:768 nt" :  : "s"(Vb) : "memory","scc","v43","v42","s98","s99");
  { u32 ln = LANE(), fa = 16384u + (ln>>4)*36864u + w*32u + (ln&15u)*2u;
  { u32x4 f = frag16(fa + 0u, 128u); asm volatile("v_accvgpr_write_b32 a224, %0\n\tv_accvgpr_write_b32 a225, %1\n\tv_accvgpr_write_b32 a226, %2\n\tv_accvgpr_write_b32 a227, %3" :  : "v"(f[0]),"v"(f[1]),"v"(f[2]),"v"(f[3]) : "memory"); }
  { u32x4 f = frag16(fa + 1024u, 128u); asm volatile("v_accvgpr_write_b32 a228, %0\n\tv_accvgpr_write_b32 a229, %1\n\tv_accvgpr_write_b32 a230, %2\n\tv_accvgpr_write_b32 a231, %3" :  : "v"(f[0]),"v"(f[1]),"v"(f[2]),"v"(f[3]) : "memory"); }
  { u32x4 f = frag16(fa + 2048u, 128u); asm volatile("v_accvgpr_write_b32 a232, %0\n\tv_accvgpr_write_b32 a233, %1\n\tv_accvgpr_write_b32 a234, %2\n\tv_accvgpr_write_b32 a235, %3" :  : "v"(f[0]),"v"(f[1]),"v"(f[2]),"v"(f[3]) : "memory"); }
  { u32x4 f = frag16(fa + 3072u, 128u); asm volatile("v_accvgpr_write_b32 a236, %0\n\tv_accvgpr_write_b32 a237, %1\n\tv_accvgpr_write_b32 a238, %2\n\tv_accvgpr_write_b32 a239, %3" :  : "v"(f[0]),"v"(f[1]),"v"(f[2]),"v"(f[3]) : "memory"); }
  { u32x4 f = frag16(fa + 3584u, 128u); asm volatile("v_mov_b32 v58, %0\n\tv_mov_b32 v59, %1" :  : "v"(f[2]),"v"(f[3]) : "memory"); }
  asm volatile("v_xor_b32 v43, 0x40000, v45\n\tglobal_load_dwordx4 v[144:147], v43, %0 offset:0 nt\n\tglobal_load_dwordx4 v[148:151], v43, %0 offset:256 nt\n\tglobal_load_dwordx4 v[152:155], v43, %0 offset:512 nt\n\tglobal_load_dwordx4 v[156:159], v43, %0 offset:768 nt" :  : "s"(Vb) : "memory","scc","v43","v42","s98","s99");
  u32 ha = 11520u + (ln&1u)*1152u + (s>>2)*288u + (ln>>4)*72u; u64 qd = pin(wsb + 1048576ull + (u64)(kvh*16384 + (s&3)*4096 + w*256 + (s>>2)*1024));
  u32x4 f0 = ld8x2(ha), f1 = ld8x2(ha+16), f2 = ld8x2(ha+32), f3 = ld8x2(ha+48); u32x2 f4 = *LDSP(u32x2, ha+64); f32x4 dq; asm volatile("s_nop 1\n\tv_mfma_f32_16x16x32_bf16 %0, %1, a[224:227], 0\n\tv_mfma_f32_16x16x32_bf16 %0, %2, a[228:231], %0\n\tv_mfma_f32_16x16x32_bf16 %0, %3, a[232:235], %0\n\tv_mfma_f32_16x16x32_bf16 %0, %4, a[236:239], %0\n\ts_nop 7\n\tv_mfma_f32_16x16x16_bf16 %0, %5, v[58:59], %0\n\ts_nop 7" : "=&v"(dq) : "v"(f0),"v"(f1),"v"(f2),"v"(f3),"v"(f4) : "memory");
  if (ln < 16u) st16(ln*16u, u32x4{__float_as_uint(dq[0]), tag, __float_as_uint(dq[1]), tag}, qd); }
  clear_slots(w, wsb, lid, xg, i32, kvh, s);
  pace(tb, 554u);
  asm volatile("v_xor_b32 v43, 0x80000, v45\n\tglobal_load_dwordx4 v[160:163], v43, %0 offset:0 nt\n\tglobal_load_dwordx4 v[164:167], v43, %0 offset:256 nt\n\tglobal_load_dwordx4 v[168:171], v43, %0 offset:512 nt\n\tglobal_load_dwordx4 v[172:175], v43, %0 offset:768 nt\n\tv_add_u32 v43, 0, v52\n\ts_add_u32 m0, %1, 11776\n\ts_nop 0\n\tglobal_load_lds_dword v43, %2\n\tv_add_u32 v43, 8064, v52\n\ts_add_u32 m0, %1, 12028\n\ts_nop 0\n\tglobal_load_lds_dword v43, %2" :  : "s"(Vb), "s"(lb), "s"(wog) : "memory","scc","v43","v42","s98","s99");
  { u32 o_ = w*2304u + LANE()*16u; asm volatile("global_load_dwordx4 a[188:191], %0, %1 offset:1024\n\tglobal_load_dwordx4 a[192:195], %0, %2 offset:1024\n\tglobal_load_dwordx4 a[196:199], %0, %3 offset:1024" :  : "v"(o_), "s"(w1p), "s"(b1p), "s"(w2p) : "memory"); }
  asm volatile("v_accvgpr_read_b32 v14, a177\n\tv_accvgpr_read_b32 v15, a181\n\tv_accvgpr_read_b32 v12, a185\n\ts_mov_b32 s84, %0\n\ts_call_b64 s[96:97], .Ltab\n\t1:\n\ts_memrealtime s[98:99]\n\ts_waitcnt lgkmcnt(0)\n\ts_sub_u32 s98, s98, %1\n\ts_cmp_lt_u32 s98, 615\n\ts_cbranch_scc1 1b\n\tv_xor_b32 v43, 0xc0000, v45\n\tglobal_load_dwordx4 v[176:179], v43, %2 offset:0 nt\n\tglobal_load_dwordx4 v[180:183], v43, %2 offset:256 nt\n\tglobal_load_dwordx4 v[184:187], v43, %2 offset:512 nt\n\tglobal_load_dwordx4 v[188:191], v43, %2 offset:768 nt\n\tv_add_u32 v43, 16128, v52\n\ts_add_u32 m0, %3, 12280\n\ts_nop 0\n\tglobal_load_lds_dword v43, %4\n\tv_add_u32 v43, 24192, v52\n\ts_add_u32 m0, %3, 12532\n\ts_nop 0\n\tglobal_load_lds_dword v43, %4\n\ts_call_b64 s[96:97], .Ltab2\n\t1:\n\ts_memrealtime s[98:99]\n\ts_waitcnt lgkmcnt(0)\n\ts_sub_u32 s98, s98, %1\n\ts_cmp_lt_u32 s98, 676\n\ts_cbranch_scc1 1b\n\tv_xor_b32 v43, 0x100000, v44\n\tglobal_load_dwordx4 v[192:195], v43, %5 offset:0 nt\n\tglobal_load_dwordx4 v[196:199], v43, %5 offset:128 nt\n\tglobal_load_dwordx4 v[200:203], v43, %5 offset:512 nt\n\tglobal_load_dwordx4 v[204:207], v43, %5 offset:640 nt\n\tv_add_u32 v43, 32256, v52\n\ts_add_u32 m0, %3, 12784\n\ts_nop 0\n\tglobal_load_lds_dword v43, %4\n\tv_add_u32 v43, 40320, v52\n\ts_add_u32 m0, %3, 13036\n\ts_nop 0\n\tglobal_load_lds_dword v43, %4" :  : "s"(hb0), "s"(tb), "s"(Vb), "s"(lb), "s"(wog), "s"(Kb) : "memory","scc","v8","v9","v10","v11","v12","v13","v14","v15","v16","v17","v18","v19","v20","v21","v22","v23","v24","v25","v26","v27","v28","v29","v30","v31","v32","v33","v34","v35","v36","v37","v38","v39","v40","v41","v42","v43","s84","s85","s86","s87","s88","s89","s90","s91","s92","s93","s94","s95","s96","s97","s98","s99");
  asm volatile("v_accvgpr_read_b32 v14, a178\n\tv_accvgpr_read_b32 v15, a182\n\tv_accvgpr_read_b32 v12, a186\n\ts_mov_b32 s84, %0\n\ts_call_b64 s[96:97], .Ltab\n\t1:\n\ts_memrealtime s[98:99]\n\ts_waitcnt lgkmcnt(0)\n\ts_sub_u32 s98, s98, %1\n\ts_cmp_lt_u32 s98, 738\n\ts_cbranch_scc1 1b\n\tv_xor_b32 v43, 0x140000, v44\n\tglobal_load_dwordx4 v[208:211], v43, %2 offset:0 nt\n\tglobal_load_dwordx4 v[212:215], v43, %2 offset:128 nt\n\tglobal_load_dwordx4 v[216:219], v43, %2 offset:512 nt\n\tglobal_load_dwordx4 v[220:223], v43, %2 offset:640 nt\n\tv_add_u32 v43, 48384, v52\n\ts_add_u32 m0, %3, 13288\n\ts_nop 0\n\tglobal_load_lds_dword v43, %4\n\tv_add_u32 v43, 56448, v52\n\ts_add_u32 m0, %3, 13540\n\ts_nop 0\n\tglobal_load_lds_dword v43, %4\n\ts_call_b64 s[96:97], .Ltab2\n\t1:\n\ts_memrealtime s[98:99]\n\ts_waitcnt lgkmcnt(0)\n\ts_sub_u32 s98, s98, %1\n\ts_cmp_lt_u32 s98, 800\n\ts_cbranch_scc1 1b\n\tv_xor_b32 v43, 0x180000, v44\n\tglobal_load_dwordx4 v[224:227], v43, %2 offset:0 nt\n\tglobal_load_dwordx4 v[228:231], v43, %2 offset:128 nt\n\tglobal_load_dwordx4 v[232:235], v43, %2 offset:512 nt\n\tglobal_load_dwordx4 v[236:239], v43, %2 offset:640 nt\n\tv_add_u32 v43, 64512, v52\n\ts_add_u32 m0, %3, 13792\n\ts_nop 0\n\tglobal_load_lds_dword v43, %4\n\tv_add_u32 v43, 72576, v52\n\ts_add_u32 m0, %3, 14044\n\ts_nop 0\n\tglobal_load_lds_dword v43, %4" :  : "s"(hb0), "s"(tb), "s"(Kb), "s"(lb), "s"(wog) : "memory","scc","v8","v9","v10","v11","v12","v13","v14","v15","v16","v17","v18","v19","v20","v21","v22","v23","v24","v25","v26","v27","v28","v29","v30","v31","v32","v33","v34","v35","v36","v37","v38","v39","v40","v41","v42","v43","s84","s85","s86","s87","s88","s89","s90","s91","s92","s93","s94","s95","s96","s97","s98","s99");
  { u32 o_ = w*2304u + LANE()*4u; asm volatile("global_load_dword a200, %0, %1 offset:2048\n\tglobal_load_dword a201, %0, %2 offset:2048\n\tglobal_load_dword a202, %0, %3 offset:2048" :  : "v"(o_), "s"(w1p), "s"(b1p), "s"(w2p) : "memory"); }
  asm volatile("v_accvgpr_read_b32 v14, a179\n\tv_accvgpr_read_b32 v15, a183\n\tv_accvgpr_read_b32 v12, a187\n\ts_mov_b32 s84, %0\n\ts_call_b64 s[96:97], .Ltab\n\t1:\n\ts_memrealtime s[98:99]\n\ts_waitcnt lgkmcnt(0)\n\ts_sub_u32 s98, s98, %1\n\ts_cmp_lt_u32 s98, 861\n\ts_cbranch_scc1 1b\n\tv_xor_b32 v43, 0x1c0000, v44\n\ts_add_u32 m0, %3, 16384\n\ts_nop 0\n\tglobal_load_lds_dwordx4 v43, %2 offset:0 nt\n\ts_add_u32 m0, %3, 17280\n\ts_nop 0\n\tglobal_load_lds_dwordx4 v43, %2 offset:128 nt\n\ts_add_u32 m0, %3, 17920\n\ts_nop 0\n\tglobal_load_lds_dwordx4 v43, %2 offset:512 nt\n\ts_add_u32 m0, %3, 18816\n\ts_nop 0\n\tglobal_load_lds_dwordx4 v43, %2 offset:640 nt\n\tv_add_u32 v43, 80640, v52\n\ts_add_u32 m0, %3, 14296\n\ts_nop 0\n\tglobal_load_lds_dword v43, %4\n\tv_add_u32 v43, 88704, v52\n\ts_add_u32 m0, %3, 14548\n\ts_nop 0\n\tglobal_load_lds_dword v43, %4\n\ts_call_b64 s[96:97], .Ltab2\n\t1:\n\ts_memrealtime s[98:99]\n\ts_waitcnt lgkmcnt(0)\n\ts_sub_u32 s98, s98, %1\n\ts_cmp_lt_u32 s98, 922\n\ts_cbranch_scc1 1b\n\tv_xor_b32 v43, 0x100000, v45\n\tglobal_load_dwordx4 a[0:3], v43, %5 offset:0 nt\n\tglobal_load_dwordx4 a[4:7], v43, %5 offset:256 nt\n\tglobal_load_dwordx4 a[8:11], v43, %5 offset:512 nt\n\tglobal_load_dwordx4 a[12:15], v43, %5 offset:768 nt\n\tv_add_u32 v43, 96768, v52\n\ts_add_u32 m0, %3, 14800\n\ts_nop 0\n\tglobal_load_lds_dword v43, %4\n\tv_add_u32 v43, 104832, v52\n\ts_add_u32 m0, %3, 15052\n\ts_nop 0\n\tglobal_load_lds_dword v43, %4" :  : "s"(hb0), "s"(tb), "s"(Kb), "s"(lb), "s"(wog), "s"(Vb) : "memory","scc","v8","v9","v10","v11","v12","v13","v14","v15","v16","v17","v18","v19","v20","v21","v22","v23","v24","v25","v26","v27","v28","v29","v30","v31","v32","v33","v34","v35","v36","v37","v38","v39","v40","v41","v42","v43","s84","s85","s86","s87","s88","s89","s90","s91","s92","s93","s94","s95","s96","s97","s98","s99");
  asm volatile("s_waitcnt vmcnt(39)" :  :  : "memory");
  asm volatile("v_accvgpr_read_b32 v14, a188\n\tv_accvgpr_read_b32 v15, a192\n\tv_accvgpr_read_b32 v12, a196\n\ts_mov_b32 s84, %0\n\ts_call_b64 s[96:97], .Ltab\n\t1:\n\ts_memrealtime s[98:99]\n\ts_waitcnt lgkmcnt(0)\n\ts_sub_u32 s98, s98, %1\n\ts_cmp_lt_u32 s98, 984\n\ts_cbranch_scc1 1b\n\tv_xor_b32 v43, 0x140000, v45\n\tglobal_load_dwordx4 a[16:19], v43, %2 offset:0 nt\n\tglobal_load_dwordx4 a[20:23], v43, %2 offset:256 nt\n\tglobal_load_dwordx4 a[24:27], v43, %2 offset:512 nt\n\tglobal_load_dwordx4 a[28:31], v43, %2 offset:768 nt\n\tv_add_u32 v43, 112896, v52\n\ts_add_u32 m0, %3, 15304\n\ts_nop 0\n\tglobal_load_lds_dword v43, %4\n\tv_add_u32 v43, 120960, v52\n\ts_add_u32 m0, %3, 15556\n\ts_nop 0\n\tglobal_load_lds_dword v43, %4\n\ts_call_b64 s[96:97], .Ltab2\n\t1:\n\ts_memrealtime s[98:99]\n\ts_waitcnt lgkmcnt(0)\n\ts_sub_u32 s98, s98, %1\n\ts_cmp_lt_u32 s98, 1046\n\ts_cbranch_scc1 1b\n\tv_xor_b32 v43, 0x180000, v45\n\tglobal_load_dwordx4 a[32:35], v43, %2 offset:0 nt\n\tglobal_load_dwordx4 a[36:39], v43, %2 offset:256 nt\n\tglobal_load_dwordx4 a[40:43], v43, %2 offset:512 nt\n\tglobal_load_dwordx4 a[44:47], v43, %2 offset:768 nt\n\tv_add_u32 v43, 129024, v52\n\ts_add_u32 m0, %3, 15808\n\ts_nop 0\n\tglobal_load_lds_dword v43, %4\n\tv_add_u32 v43, 137088, v52\n\ts_add_u32 m0, %3, 16060\n\ts_nop 0\n\tglobal_load_lds_dword v43, %4" :  : "s"(hb0), "s"(tb), "s"(Vb), "s"(lb), "s"(wog) : "memory","scc","v8","v9","v10","v11","v12","v13","v14","v15","v16","v17","v18","v19","v20","v21","v22","v23","v24","v25","v26","v27","v28","v29","v30","v31","v32","v33","v34","v35","v36","v37","v38","v39","v40","v41","v42","v43","s84","s85","s86","s87","s88","s89","s90","s91","s92","s93","s94","s95","s96","s97","s98","s99");
  asm volatile("v_accvgpr_read_b32 v14, a189\n\tv_accvgpr_read_b32 v15, a193\n\tv_accvgpr_read_b32 v12, a197\n\ts_mov_b32 s84, %0\n\ts_call_b64 s[96:97], .Ltab\n\t1:\n\ts_memrealtime s[98:99]\n\ts_waitcnt lgkmcnt(0)\n\ts_sub_u32 s98, s98, %1\n\ts_cmp_lt_u32 s98, 1107\n\ts_cbranch_scc1 1b\n\tv_xor_b32 v43, 0x1c0000, v45\n\tglobal_load_dwordx4 a[48:51], v43, %2 offset:0 nt\n\tglobal_load_dwordx4 a[52:55], v43, %2 offset:256 nt\n\tglobal_load_dwordx4 a[56:59], v43, %2 offset:512 nt\n\tglobal_load_dwordx4 a[60:63], v43, %2 offset:768 nt\n\tv_add_u32 v43, 139392, v52\n\ts_add_u32 m0, %3, 16132\n\tv_min_u32 v43, 146336, v43\n\ts_bitset0_b64 exec, 63\n\tglobal_load_lds_dword v43, %4\n\ts_bitset1_b64 exec, 63\n\ts_call_b64 s[96:97], .Ltab2\n\t1:\n\ts_memrealtime s[98:99]\n\ts_waitcnt lgkmcnt(0)\n\ts_sub_u32 s98, s98, %1\n\ts_cmp_lt_u32 s98, 1168\n\ts_cbranch_scc1 1b\n\tv_xor_b32 v43, 0x200000, v44\n\ts_add_u32 m0, %3, 20480\n\ts_nop 0\n\tglobal_load_lds_dwordx4 v43, %5 offset:0 nt\n\ts_add_u32 m0, %3, 21376\n\ts_nop 0\n\tglobal_load_lds_dwordx4 v43, %5 offset:128 nt\n\ts_add_u32 m0, %3, 22016\n\ts_nop 0\n\tglobal_load_lds_dwordx4 v43, %5 offset:512 nt\n\ts_add_u32 m0, %3, 22912\n\ts_nop 0\n\tglobal_load_lds_dwordx4 v43, %5 offset:640 nt" :  : "s"(hb0), "s"(tb), "s"(Vb), "s"(lb), "s"(wog), "s"(Kb) : "memory","scc","v8","v9","v10","v11","v12","v13","v14","v15","v16","v17","v18","v19","v20","v21","v22","v23","v24","v25","v26","v27","v28","v29","v30","v31","v32","v33","v34","v35","v36","v37","v38","v39","v40","v41","v42","v43","s84","s85","s86","s87","s88","s89","s90","s91","s92","s93","s94","s95","s96","s97","s98","s99");
  { u32 o_ = LANE()*16u; asm volatile("global_load_dwordx4 a[240:243], %0, %1 offset:0 \n\tglobal_load_dwordx4 a[244:247], %0, %1 offset:1024 \n\tglobal_load_dwordx4 a[248:251], %0, %1 offset:2048 \n\tglobal_load_dwordx4 a[252:255], %0, %1 offset:3072 " :  : "v"(o_), "s"(wsb + 1048576ull + (u64)(kvh*16384 + w*4096)) : "memory"); }
  asm volatile("v_accvgpr_read_b32 v14, a190\n\tv_accvgpr_read_b32 v15, a194\n\tv_accvgpr_read_b32 v12, a198\n\ts_mov_b32 s84, %0\n\ts_call_b64 s[96:97], .Ltab\n\t1:\n\ts_memrealtime s[98:99]\n\ts_waitcnt lgkmcnt(0)\n\ts_sub_u32 s98, s98, %1\n\ts_cmp_lt_u32 s98, 1230\n\ts_cbranch_scc1 1b\n\tv_xor_b32 v43, 0x240000, v44\n\ts_add_u32 m0, %3, 24576\n\ts_nop 0\n\tglobal_load_lds_dwordx4 v43, %2 offset:0 nt\n\ts_add_u32 m0, %3, 25472\n\ts_nop 0\n\tglobal_load_lds_dwordx4 v43, %2 offset:128 nt\n\ts_add_u32 m0, %3, 26112\n\ts_nop 0\n\tglobal_load_lds_dwordx4 v43, %2 offset:512 nt\n\ts_add_u32 m0, %3, 27008\n\ts_nop 0\n\tglobal_load_lds_dwordx4 v43, %2 offset:640 nt\n\ts_call_b64 s[96:97], .Ltab2\n\t1:\n\ts_memrealtime s[98:99]\n\ts_waitcnt lgkmcnt(0)\n\ts_sub_u32 s98, s98, %1\n\ts_cmp_lt_u32 s98, 1292\n\ts_cbranch_scc1 1b\n\tv_xor_b32 v43, 0x280000, v44\n\ts_add_u32 m0, %3, 28672\n\ts_nop 0\n\tglobal_load_lds_dwordx4 v43, %2 offset:0 nt\n\ts_add_u32 m0, %3, 29568\n\ts_nop 0\n\tglobal_load_lds_dwordx4 v43, %2 offset:128 nt\n\ts_add_u32 m0, %3, 30208\n\ts_nop 0\n\tglobal_load_lds_dwordx4 v43, %2 offset:512 nt\n\ts_add_u32 m0, %3, 31104\n\ts_nop 0\n\tglobal_load_lds_dwordx4 v43, %2 offset:640 nt" :  : "s"(hb0), "s"(tb), "s"(Kb), "s"(lb) : "memory","scc","v8","v9","v10","v11","v12","v13","v14","v15","v16","v17","v18","v19","v20","v21","v22","v23","v24","v25","v26","v27","v28","v29","v30","v31","v32","v33","v34","v35","v36","v37","v38","v39","v40","v41","v42","v43","s84","s85","s86","s87","s88","s89","s90","s91","s92","s93","s94","s95","s96","s97","s98","s99");
  asm volatile("v_accvgpr_read_b32 v14, a191\n\tv_accvgpr_read_b32 v15, a195\n\tv_accvgpr_read_b32 v12, a199\n\ts_mov_b32 s84, %0\n\ts_call_b64 s[96:97], .Ltab\n\t1:\n\ts_memrealtime s[98:99]\n\ts_waitcnt lgkmcnt(0)\n\ts_sub_u32 s98, s98, %1\n\ts_cmp_lt_u32 s98, 1353\n\ts_cbranch_scc1 1b\n\tv_xor_b32 v43, 0x2c0000, v44\n\ts_add_u32 m0, %3, 32768\n\ts_nop 0\n\tglobal_load_lds_dwordx4 v43, %2 offset:0 nt\n\ts_add_u32 m0, %3, 33664\n\ts_nop 0\n\tglobal_load_lds_dwordx4 v43, %2 offset:128 nt\n\ts_add_u32 m0, %3, 34304\n\ts_nop 0\n\tglobal_load_lds_dwordx4 v43, %2 offset:512 nt\n\ts_add_u32 m0, %3, 35200\n\ts_nop 0\n\tglobal_load_lds_dwordx4 v43, %2 offset:640 nt\n\ts_call_b64 s[96:97], .Ltab2\n\t1:\n\ts_memrealtime s[98:99]\n\ts_waitcnt lgkmcnt(0)\n\ts_sub_u32 s98, s98, %1\n\ts_cmp_lt_u32 s98, 1414\n\ts_cbranch_scc1 1b\n\tv_xor_b32 v43, 0x200000, v45\n\tglobal_load_dwordx4 a[64:67], v43, %4 offset:0 nt\n\tglobal_load_dwordx4 a[68:71], v43, %4 offset:256 nt\n\tglobal_load_dwordx4 a[72:75], v43, %4 offset:512 nt\n\tglobal_load_dwordx4 a[76:79], v43, %4 offset:768 nt" :  : "s"(hb0), "s"(tb), "s"(Kb), "s"(lb), "s"(Vb) : "memory","scc","v8","v9","v10","v11","v12","v13","v14","v15","v16","v17","v18","v19","v20","v21","v22","v23","v24","v25","v26","v27","v28","v29","v30","v31","v32","v33","v34","v35","v36","v37","v38","v39","v40","v41","v42","v43","s84","s85","s86","s87","s88","s89","s90","s91","s92","s93","s94","s95","s96","s97","s98","s99");
  { u32x4 a0, a1, a2, a3;
  asm volatile("s_waitcnt vmcnt(16)\n\tv_accvgpr_read_b32 %0, a240\n\tv_accvgpr_read_b32 %1, a241\n\tv_accvgpr_read_b32 %2, a242\n\tv_accvgpr_read_b32 %3, a243\n\tv_accvgpr_read_b32 %4, a244\n\tv_accvgpr_read_b32 %5, a245\n\tv_accvgpr_read_b32 %6, a246\n\tv_accvgpr_read_b32 %7, a247\n\tv_accvgpr_read_b32 %8, a248\n\tv_accvgpr_read_b32 %9, a249\n\tv_accvgpr_read_b32 %10, a250\n\tv_accvgpr_read_b32 %11, a251\n\tv_accvgpr_read_b32 %12, a252\n\tv_accvgpr_read_b32 %13, a253\n\tv_accvgpr_read_b32 %14, a254\n\tv_accvgpr_read_b32 %15, a255" : "=v"(a0[0]),"=v"(a0[1]),"=v"(a0[2]),"=v"(a0[3]),"=v"(a1[0]),"=v"(a1[1]),"=v"(a1[2]),"=v"(a1[3]),"=v"(a2[0]),"=v"(a2[1]),"=v"(a2[2]),"=v"(a2[3]),"=v"(a3[0]),"=v"(a3[1]),"=v"(a3[2]),"=v"(a3[3]) :  : "memory");
  q1_poll(a0, a1, a2, a3, w, tag, wsb + 1048576ull + (u64)(kvh*16384 + w*4096)); }
  BAR();
  asm volatile("s_waitcnt vmcnt(53)" :  :  : "memory");
  asm volatile("v_accvgpr_read_b32 v14, a200\n\tv_accvgpr_read_b32 v15, a201\n\tv_accvgpr_read_b32 v12, a202\n\ts_mov_b32 s84, %0\n\ts_call_b64 s[96:97], .Ltab\n\t1:\n\ts_memrealtime s[98:99]\n\ts_waitcnt lgkmcnt(0)\n\ts_sub_u32 s98, s98, %1\n\ts_cmp_lt_u32 s98, 1476\n\ts_cbranch_scc1 1b\n\tv_xor_b32 v43, 0x240000, v45\n\tglobal_load_dwordx4 a[80:83], v43, %2 offset:0 nt\n\tglobal_load_dwordx4 a[84:87], v43, %2 offset:256 nt\n\tglobal_load_dwordx4 a[88:91], v43, %2 offset:512 nt\n\tglobal_load_dwordx4 a[92:95], v43, %2 offset:768 nt\n\ts_call_b64 s[96:97], .Ltab2\n\t1:\n\ts_memrealtime s[98:99]\n\ts_waitcnt lgkmcnt(0)\n\ts_sub_u32 s98, s98, %1\n\ts_cmp_lt_u32 s98, 1538\n\ts_cbranch_scc1 1b\n\tv_xor_b32 v43, 0x280000, v45\n\tglobal_load_dwordx4 a[96:99], v43, %2 offset:0 nt\n\tglobal_load_dwordx4 a[100:103], v43, %2 offset:256 nt\n\tglobal_load_dwordx4 a[104:107], v43, %2 offset:512 nt\n\tglobal_load_dwordx4 a[108:111], v43, %2 offset:768 nt" :  : "s"(hb0), "s"(tb), "s"(Vb) : "memory","scc","v8","v9","v10","v11","v12","v13","v14","v15","v16","v17","v18","v19","v20","v21","v22","v23","v24","v25","v26","v27","v28","v29","v30","v31","v32","v33","v34","v35","v36","v37","v38","v39","v40","v41","v42","v43","s84","s85","s86","s87","s88","s89","s90","s91","s92","s93","s94","s95","s96","s97","s98","s99");
  { float acc[16];
  asm volatile("v_mov_b32 v40, 0\n\tv_mov_b32 v41, 0\n\tv_accvgpr_read_b32 v34, a176\n\tv_accvgpr_read_b32 v35, a180\n\tv_accvgpr_read_b32 v36, a184\n\tv_accvgpr_read_b32 v37, a177\n\tv_accvgpr_read_b32 v38, a181\n\tv_accvgpr_read_b32 v39, a185\n\tv_dot2c_f32_bf16 v40, v34, v36\n\tv_dot2c_f32_bf16 v41, v35, v36\n\tv_accvgpr_read_b32 v34, a178\n\tv_accvgpr_read_b32 v35, a182\n\tv_accvgpr_read_b32 v36, a186\n\tv_dot2c_f32_bf16 v40, v37, v39\n\tv_dot2c_f32_bf16 v41, v38, v39\n\tv_accvgpr_read_b32 v37, a179\n\tv_accvgpr_read_b32 v38, a183\n\tv_accvgpr_read_b32 v39, a187\n\tv_dot2c_f32_bf16 v40, v34, v36\n\tv_dot2c_f32_bf16 v41, v35, v36\n\tv_accvgpr_read_b32 v34, a188\n\tv_accvgpr_read_b32 v35, a192\n\tv_accvgpr_read_b32 v36, a196\n\tv_dot2c_f32_bf16 v40, v37, v39\n\tv_dot2c_f32_bf16 v41, v38, v39\n\tv_accvgpr_read_b32 v37, a189\n\tv_accvgpr_read_b32 v38, a193\n\tv_accvgpr_read_b32 v39, a197\n\tv_dot2c_f32_bf16 v40, v34, v36\n\tv_dot2c_f32_bf16 v41, v35, v36\n\tv_accvgpr_read_b32 v34, a190\n\tv_accvgpr_read_b32 v35, a194\n\tv_accvgpr_read_b32 v36, a198\n\tv_dot2c_f32_bf16 v40, v37, v39\n\tv_dot2c_f32_bf16 v41, v38, v39\n\tv_accvgpr_read_b32 v37, a191\n\tv_accvgpr_read_b32 v38, a195\n\tv_accvgpr_read_b32 v39, a199\n\tv_dot2c_f32_bf16 v40, v34, v36\n\tv_dot2c_f32_bf16 v41, v35, v36\n\tv_accvgpr_read_b32 v34, a200\n\tv_accvgpr_read_b32 v35, a201\n\tv_accvgpr_read_b32 v36, a202\n\tv_dot2c_f32_bf16 v40, v37, v39\n\tv_dot2c_f32_bf16 v41, v38, v39\n\ts_nop 0\n\tv_dot2c_f32_bf16 v40, v34, v36\n\tv_dot2c_f32_bf16 v41, v35, v36\n\ts_nop 1\n\tv_mul_f32 v40, 0.5, v40\n\ts_cmp_lt_u32 %16, 0x3a800000\n\ts_cselect_b32 s98, 0x3a800000, 0\n\ts_or_b32 s99, %16, s98\n\tv_mul_f32 v41, 0.5, v41\n\tv_fma_f32 v39, s98, v40, v41\n\tv_fma_f32 v38, -s98, v40, v41\n\tv_add_f32 %0, v38, v240\n\tv_add_f32 %1, v39, v241\n\tv_add_f32 %2, v38, v242\n\tv_add_f32 %3, v39, v243\n\tv_add_f32 %4, v38, v244\n\tv_add_f32 %5, v39, v245\n\tv_add_f32 %6, v38, v246\n\tv_add_f32 %7, v39, v247\n\tv_add_f32 %8, v38, v248\n\tv_add_f32 %9, v39, v249\n\tv_add_f32 %10, v38, v250\n\tv_add_f32 %11, v39, v251\n\tv_add_f32 %12, v38, v252\n\tv_add_f32 %13, v39, v253\n\tv_add_f32 %14, v38, v254\n\tv_add_f32 %15, v39, v255\n\ts_add_u32 s98, s99, 0x0\n\tv_fma_f32 %0, s98, v40, %0\n\tv_fma_f32 %1, -s98, v40, %1\n\ts_add_u32 s98, s99, 0x10000\n\tv_fma_f32 %2, s98, v40, %2\n\tv_fma_f32 %3, -s98, v40, %3\n\ts_add_u32 s98, s99, 0x20000\n\tv_fma_f32 %4, s98, v40, %4\n\tv_fma_f32 %5, -s98, v40, %5\n\ts_add_u32 s98, s99, 0x30000\n\tv_fma_f32 %6, s98, v40, %6\n\tv_fma_f32 %7, -s98, v40, %7\n\ts_add_u32 s98, s99, 0x40000\n\tv_fma_f32 %8, s98, v40, %8\n\tv_fma_f32 %9, -s98, v40, %9\n\ts_add_u32 s98, s99, 0x50000\n\tv_fma_f32 %10, s98, v40, %10\n\tv_fma_f32 %11, -s98, v40, %11\n\ts_add_u32 s98, s99, 0x60000\n\tv_fma_f32 %12, s98, v40, %12\n\tv_fma_f32 %13, -s98, v40, %13\n\ts_add_u32 s98, s99, 0x70000\n\tv_fma_f32 %14, s98, v40, %14\n\tv_fma_f32 %15, -s98, v40, %15" : "=&v"(acc[0]),"=&v"(acc[1]),"=&v"(acc[2]),"=&v"(acc[3]),"=&v"(acc[4]),"=&v"(acc[5]),"=&v"(acc[6]),"=&v"(acc[7]),"=&v"(acc[8]),"=&v"(acc[9]),"=&v"(acc[10]),"=&v"(acc[11]),"=&v"(acc[12]),"=&v"(acc[13]),"=&v"(acc[14]),"=&v"(acc[15]) : "s"(hb0) : "memory","scc","s98","s99","v34","v35","v36","v37","v38","v39","v40","v41");
  table_publish(acc, w, b2f, tag, wsb + 36864ull + (u64)lid*64u); }
  pace(tb, 1599u);
  asm volatile("v_xor_b32 v43, 0x2c0000, v45\n\tglobal_load_dwordx4 a[112:115], v43, %0 offset:0 nt\n\tglobal_load_dwordx4 a[116:119], v43, %0 offset:256 nt\n\tglobal_load_dwordx4 a[120:123], v43, %0 offset:512 nt\n\tglobal_load_dwordx4 a[124:127], v43, %0 offset:768 nt" :  : "s"(Vb) : "memory","scc","v43","v42","s98","s99");
  asm volatile("v_mov_b32 v40, 7264\n\ts_mov_b32 vcc_lo, 0x11111111\n\ts_mov_b32 vcc_hi, 0x11111111\n\tv_cndmask_b32 v4, v40, v47, vcc\n\ts_lshl_b64 vcc, vcc, 1\n\tv_cndmask_b32 v5, v40, v47, vcc\n\ts_lshl_b64 vcc, vcc, 1\n\tv_cndmask_b32 v6, v40, v47, vcc\n\ts_lshl_b64 vcc, vcc, 1\n\tv_cndmask_b32 v7, v40, v47, vcc\n\tds_read_b128 v[8:11], v4 offset:8704\n\tds_read_b128 v[12:15], v4 offset:8720\n\tds_read_b128 v[16:19], v5 offset:8704\n\tds_read_b128 v[20:23], v5 offset:8720\n\tds_read_b128 v[24:27], v6 offset:8704\n\tds_read_b128 v[28:31], v6 offset:8720\n\tds_read_b128 v[32:35], v7 offset:8704\n\tds_read_b128 v[36:39], v7 offset:8720\n\ts_waitcnt vmcnt(63)\n\ts_waitcnt lgkmcnt(0)\n\tv_mfma_f32_16x16x32_bf16 v[4:7], v[64:67], v[8:11], 0\n\tv_mfma_f32_16x16x32_bf16 v[40:43], v[72:75], v[8:11], 0\n\tv_mfma_f32_16x16x32_bf16 v[4:7], v[68:71], v[12:15], v[4:7]\n\tv_mfma_f32_16x16x32_bf16 v[40:43], v[76:79], v[12:15], v[40:43]\n\tv_mfma_f32_16x16x32_bf16 v[4:7], v[80:83], v[16:19], v[4:7]\n\tv_mfma_f32_16x16x32_bf16 v[40:43], v[88:91], v[16:19], v[40:43]\n\tv_mfma_f32_16x16x32_bf16 v[4:7], v[84:87], v[20:23], v[4:7]\n\tv_mfma_f32_16x16x32_bf16 v[40:43], v[92:95], v[20:23], v[40:43]\n\tv_mfma_f32_16x16x32_bf16 v[4:7], v[96:99], v[24:27], v[4:7]\n\tv_mfma_f32_16x16x32_bf16 v[40:43], v[104:107], v[24:27], v[40:43]\n\tv_mfma_f32_16x16x32_bf16 v[4:7], v[100:103], v[28:31], v[4:7]\n\tv_mfma_f32_16x16x32_bf16 v[40:43], v[108:111], v[28:31], v[40:43]\n\tv_mfma_f32_16x16x32_bf16 v[4:7], v[112:115], v[32:35], v[4:7]\n\tv_mfma_f32_16x16x32_bf16 v[40:43], v[120:123], v[32:35], v[40:43]\n\tv_mfma_f32_16x16x32_bf16 v[4:7], v[116:119], v[36:39], v[4:7]\n\tv_mfma_f32_16x16x32_bf16 v[40:43], v[124:127], v[36:39], v[40:43]\n\t1:\n\ts_memrealtime s[98:99]\n\ts_waitcnt lgkmcnt(0)\n\ts_sub_u32 s98, s98, %1\n\ts_cmp_lt_u32 s98, 1660\n\ts_cbranch_scc1 1b\n\tv_xor_b32 v14, 0x300000, v44\n\ts_add_u32 m0, %3, 0\n\ts_nop 0\n\tglobal_load_lds_dwordx4 v14, %2 offset:0 nt\n\ts_add_u32 m0, %3, 896\n\ts_nop 0\n\tglobal_load_lds_dwordx4 v14, %2 offset:128 nt\n\ts_add_u32 m0, %3, 1536\n\ts_nop 0\n\tglobal_load_lds_dwordx4 v14, %2 offset:512 nt\n\ts_add_u32 m0, %3, 2432\n\ts_nop 0\n\tglobal_load_lds_dwordx4 v14, %2 offset:640 nt\n\ts_nop 0\n\tv_add_f32_dpp v8, v5, v4 row_ror:8 row_mask:0xf bank_mask:0x3\n\tv_add_f32_dpp v9, v7, v6 row_ror:8 row_mask:0xf bank_mask:0x3\n\tv_add_f32_dpp v10, v41, v40 row_ror:8 row_mask:0xf bank_mask:0x3\n\tv_add_f32_dpp v11, v43, v42 row_ror:8 row_mask:0xf bank_mask:0x3\n\tv_add_f32_dpp v8, v4, v5 row_ror:8 row_mask:0xf bank_mask:0xc\n\tv_add_f32_dpp v9, v6, v7 row_ror:8 row_mask:0xf bank_mask:0xc\n\tv_add_f32_dpp v10, v40, v41 row_ror:8 row_mask:0xf bank_mask:0xc\n\tv_add_f32_dpp v11, v42, v43 row_ror:8 row_mask:0xf bank_mask:0xc\n\tv_max3_f32 v12, v8, v9, v10\n\tv_max_f32 v12, v12, v11\n\ts_nop 1\n\tv_max_f32_dpp v12, v12, v12 quad_perm:[1,0,3,2] row_mask:0xf bank_mask:0xf\n\ts_nop 1\n\tv_max_f32_dpp v12, v12, v12 quad_perm:[2,3,0,1] row_mask:0xf bank_mask:0xf\n\tv_mov_b32 v13, v12\n\ts_nop 1\n\tv_permlane16_swap_b32 v12, v13\n\ts_nop 0\n\tv_max_f32 v12, v12, v13\n\tv_mov_b32 v13, v12\n\ts_nop 1\n\tv_permlane32_swap_b32 v12, v13\n\ts_nop 0\n\tv_max_f32 v12, v12, v13\n\tv_mul_f32 v15, %0, v12\n\tv_fma_f32 v8, v8, %0, -v15\n\tv_fma_f32 v9, v9, %0, -v15\n\tv_fma_f32 v10, v10, %0, -v15\n\tv_fma_f32 v11, v11, %0, -v15\n\tv_exp_f32 v8, v8\n\tv_exp_f32 v9, v9\n\tv_exp_f32 v10, v10\n\tv_exp_f32 v11, v11\n\tv_mov_b32 v56, v12\n\tv_cvt_pk_bf16_f32 v4, v8, v8\n\tv_cvt_pk_bf16_f32 v5, v9, v9\n\tv_cvt_pk_bf16_f32 v6, v10, v10\n\tv_cvt_pk_bf16_f32 v7, v11, v11\n\tv_add_f32 v8, v8, v9\n\tv_add_f32 v10, v10, v11\n\tv_add_f32 v57, v8, v10\n\tv_and_b32_dpp v16, v4, v60 quad_perm:[0,0,0,0] row_mask:0xf bank_mask:0xf\n\tv_and_b32_dpp v17, v4, v61 quad_perm:[0,0,0,0] row_mask:0xf bank_mask:0xf\n\tv_and_b32_dpp v18, v4, v62 quad_perm:[0,0,0,0] row_mask:0xf bank_mask:0xf\n\tv_and_b32_dpp v19, v4, v63 quad_perm:[0,0,0,0] row_mask:0xf bank_mask:0xf\n\tv_and_b32_dpp v20, v5, v60 quad_perm:[0,0,0,0] row_mask:0xf bank_mask:0xf\n\tv_and_b32_dpp v21, v5, v61 quad_perm:[0,0,0,0] row_mask:0xf bank_mask:0xf\n\tv_and_b32_dpp v22, v5, v62 quad_perm:[0,0,0,0] row_mask:0xf bank_mask:0xf\n\tv_and_b32_dpp v23, v5, v63 quad_perm:[0,0,0,0] row_mask:0xf bank_mask:0xf\n\ts_waitcnt vmcnt(63)\n\tv_mfma_f32_16x16x32_bf16 v[48:51], v[128:131], v[16:19], 0\n\tv_and_b32_dpp v16, v6, v60 quad_perm:[0,0,0,0] row_mask:0xf bank_mask:0xf\n\tv_and_b32_dpp v17, v6, v61 quad_perm:[0,0,0,0] row_mask:0xf bank_mask:0xf\n\tv_and_b32_dpp v18, v6, v62 quad_perm:[0,0,0,0] row_mask:0xf bank_mask:0xf\n\tv_and_b32_dpp v19, v6, v63 quad_perm:[0,0,0,0] row_mask:0xf bank_mask:0xf\n\tv_mfma_f32_16x16x32_bf16 v[48:51], v[132:135], v[20:23], v[48:51]\n\tv_and_b32_dpp v20, v7, v60 quad_perm:[0,0,0,0] row_mask:0xf bank_mask:0xf\n\tv_and_b32_dpp v21, v7, v61 quad_perm:[0,0,0,0] row_mask:0xf bank_mask:0xf\n\tv_and_b32_dpp v22, v7, v62 quad_perm:[0,0,0,0] row_mask:0xf bank_mask:0xf\n\tv_and_b32_dpp v23, v7, v63 quad_perm:[0,0,0,0] row_mask:0xf bank_mask:0xf\n\tv_mfma_f32_16x16x32_bf16 v[48:51], v[136:139], v[16:19], v[48:51]\n\tv_and_b32_dpp v16, v4, v60 quad_perm:[1,1,1,1] row_mask:0xf bank_mask:0xf\n\tv_and_b32_dpp v17, v4, v61 quad_perm:[1,1,1,1] row_mask:0xf bank_mask:0xf\n\tv_and_b32_dpp v18, v4, v62 quad_perm:[1,1,1,1] row_mask:0xf bank_mask:0xf\n\tv_and_b32_dpp v19, v4, v63 quad_perm:[1,1,1,1] row_mask:0xf bank_mask:0xf\n\tv_mfma_f32_16x16x32_bf16 v[48:51], v[140:143], v[20:23], v[48:51]\n\tv_and_b32_dpp v20, v5, v60 quad_perm:[1,1,1,1] row_mask:0xf bank_mask:0xf\n\tv_and_b32_dpp v21, v5, v61 quad_perm:[1,1,1,1] row_mask:0xf bank_mask:0xf\n\tv_and_b32_dpp v22, v5, v62 quad_perm:[1,1,1,1] row_mask:0xf bank_mask:0xf\n\tv_and_b32_dpp v23, v5, v63 quad_perm:[1,1,1,1] row_mask:0xf bank_mask:0xf\n\ts_waitcnt vmcnt(63)\n\tv_mfma_f32_16x16x32_bf16 v[48:51], v[144:147], v[16:19], v[48:51]\n\tv_and_b32_dpp v16, v6, v60 quad_perm:[1,1,1,1] row_mask:0xf bank_mask:0xf\n\tv_and_b32_dpp v17, v6, v61 quad_perm:[1,1,1,1] row_mask:0xf bank_mask:0xf\n\tv_and_b32_dpp v18, v6, v62 quad_perm:[1,1,1,1] row_mask:0xf bank_mask:0xf\n\tv_and_b32_dpp v19, v6, v63 quad_perm:[1,1,1,1] row_mask:0xf bank_mask:0xf\n\tv_mfma_f32_16x16x32_bf16 v[48:51], v[148:151], v[20:23], v[48:51]\n\tv_and_b32_dpp v20, v7, v60 quad_perm:[1,1,1,1] row_mask:0xf bank_mask:0xf\n\tv_and_b32_dpp v21, v7, v61 quad_perm:[1,1,1,1] row_mask:0xf bank_mask:0xf\n\tv_and_b32_dpp v22, v7, v62 quad_perm:[1,1,1,1] row_mask:0xf bank_mask:0xf\n\tv_and_b32_dpp v23, v7, v63 quad_perm:[1,1,1,1] row_mask:0xf bank_mask:0xf\n\tv_mfma_f32_16x16x32_bf16 v[48:51], v[152:155], v[16:19], v[48:51]\n\tv_and_b32_dpp v16, v4, v60 quad_perm:[2,2,2,2] row_mask:0xf bank_mask:0xf\n\tv_and_b32_dpp v17, v4, v61 quad_perm:[2,2,2,2] row_mask:0xf bank_mask:0xf\n\tv_and_b32_dpp v18, v4, v62 quad_perm:[2,2,2,2] row_mask:0xf bank_mask:0xf\n\tv_and_b32_dpp v19, v4, v63 quad_perm:[2,2,2,2] row_mask:0xf bank_mask:0xf\n\tv_mfma_f32_16x16x32_bf16 v[48:51], v[156:159], v[20:23], v[48:51]\n\tv_and_b32_dpp v20, v5, v60 quad_perm:[2,2,2,2] row_mask:0xf bank_mask:0xf\n\tv_and_b32_dpp v21, v5, v61 quad_perm:[2,2,2,2] row_mask:0xf bank_mask:0xf\n\tv_and_b32_dpp v22, v5, v62 quad_perm:[2,2,2,2] row_mask:0xf bank_mask:0xf\n\tv_and_b32_dpp v23, v5, v63 quad_perm:[2,2,2,2] row_mask:0xf bank_mask:0xf\n\t1:\n\ts_memrealtime s[98:99]\n\ts_waitcnt lgkmcnt(0)\n\ts_sub_u32 s98, s98, %1\n\ts_cmp_lt_u32 s98, 1722\n\ts_cbranch_scc1 1b\n\tv_xor_b32 v14, 0x340000, v44\n\ts_add_u32 m0, %3, 4096\n\ts_nop 0\n\tglobal_load_lds_dwordx4 v14, %2 offset:0 nt\n\ts_add_u32 m0, %3, 4992\n\ts_nop 0\n\tglobal_load_lds_dwordx4 v14, %2 offset:128 nt\n\ts_add_u32 m0, %3, 5632\n\ts_nop 0\n\tglobal_load_lds_dwordx4 v14, %2 offset:512 nt\n\ts_add_u32 m0, %3, 6528\n\ts_nop 0\n\tglobal_load_lds_dwordx4 v14, %2 offset:640 nt\n\ts_waitcnt vmcnt(63)\n\tv_mfma_f32_16x16x32_bf16 v[48:51], v[160:163], v[16:19], v[48:51]\n\tv_and_b32_dpp v16, v6, v60 quad_perm:[2,2,2,2] row_mask:0xf bank_mask:0xf\n\tv_and_b32_dpp v17, v6, v61 quad_perm:[2,2,2,2] row_mask:0xf bank_mask:0xf\n\tv_and_b32_dpp v18, v6, v62 quad_perm:[2,2,2,2] row_mask:0xf bank_mask:0xf\n\tv_and_b32_dpp v19, v6, v63 quad_perm:[2,2,2,2] row_mask:0xf bank_mask:0xf\n\tv_mfma_f32_16x16x32_bf16 v[48:51], v[164:167], v[20:23], v[48:51]\n\tv_and_b32_dpp v20, v7, v60 quad_perm:[2,2,2,2] row_mask:0xf bank_mask:0xf\n\tv_and_b32_dpp v21, v7, v61 quad_perm:[2,2,2,2] row_mask:0xf bank_mask:0xf\n\tv_and_b32_dpp v22, v7, v62 quad_perm:[2,2,2,2] row_mask:0xf bank_mask:0xf\n\tv_and_b32_dpp v23, v7, v63 quad_perm:[2,2,2,2] row_mask:0xf bank_mask:0xf\n\tv_mfma_f32_16x16x32_bf16 v[48:51], v[168:171], v[16:19], v[48:51]\n\tv_and_b32_dpp v16, v4, v60 quad_perm:[3,3,3,3] row_mask:0xf bank_mask:0xf\n\tv_and_b32_dpp v17, v4, v61 quad_perm:[3,3,3,3] row_mask:0xf bank_mask:0xf\n\tv_and_b32_dpp v18, v4, v62 quad_perm:[3,3,3,3] row_mask:0xf bank_mask:0xf\n\tv_and_b32_dpp v19, v4, v63 quad_perm:[3,3,3,3] row_mask:0xf bank_mask:0xf\n\tv_mfma_f32_16x16x32_bf16 v[48:51], v[172:175], v[20:23], v[48:51]\n\tv_and_b32_dpp v20, v5, v60 quad_perm:[3,3,3,3] row_mask:0xf bank_mask:0xf\n\tv_and_b32_dpp v21, v5, v61 quad_perm:[3,3,3,3] row_mask:0xf bank_mask:0xf\n\tv_and_b32_dpp v22, v5, v62 quad_perm:[3,3,3,3] row_mask:0xf bank_mask:0xf\n\tv_and_b32_dpp v23, v5, v63 quad_perm:[3,3,3,3] row_mask:0xf bank_mask:0xf\n\ts_waitcnt vmcnt(63)\n\tv_mfma_f32_16x16x32_bf16 v[48:51], v[176:179], v[16:19], v[48:51]\n\tv_and_b32_dpp v16, v6, v60 quad_perm:[3,3,3,3] row_mask:0xf bank_mask:0xf\n\tv_and_b32_dpp v17, v6, v61 quad_perm:[3,3,3,3] row_mask:0xf bank_mask:0xf\n\tv_and_b32_dpp v18, v6, v62 quad_perm:[3,3,3,3] row_mask:0xf bank_mask:0xf\n\tv_and_b32_dpp v19, v6, v63 quad_perm:[3,3,3,3] row_mask:0xf bank_mask:0xf\n\tv_mfma_f32_16x16x32_bf16 v[48:51], v[180:183], v[20:23], v[48:51]\n\tv_and_b32_dpp v20, v7, v60 quad_perm:[3,3,3,3] row_mask:0xf bank_mask:0xf\n\tv_and_b32_dpp v21, v7, v61 quad_perm:[3,3,3,3] row_mask:0xf bank_mask:0xf\n\tv_and_b32_dpp v22, v7, v62 quad_perm:[3,3,3,3] row_mask:0xf bank_mask:0xf\n\tv_and_b32_dpp v23, v7, v63 quad_perm:[3,3,3,3] row_mask:0xf bank_mask:0xf\n\tv_mfma_f32_16x16x32_bf16 v[48:51], v[184:187], v[16:19], v[48:51]\n\ts_nop 0\n\tv_mfma_f32_16x16x32_bf16 v[48:51], v[188:191], v[20:23], v[48:51]" :  : "s"(CSC), "s"(tb), "s"(Kb), "s"(lb) : "memory","scc","vcc","s98","s99","v4","v5","v6","v7","v8","v9","v10","v11","v12","v13","v14","v15","v16","v17","v18","v19","v20","v21","v22","v23","v24","v25","v26","v27","v28","v29","v30","v31","v32","v33","v34","v35","v36","v37","v38","v39","v40","v41","v42","v43");
  pace(tb, 1784u);
  asm volatile("v_xor_b32 v43, 0x380000, v44\n\tglobal_load_dwordx4 a[128:131], v43, %0 offset:0 nt\n\tglobal_load_dwordx4 a[132:135], v43, %0 offset:128 nt\n\tglobal_load_dwordx4 a[136:139], v43, %0 offset:512 nt\n\tglobal_load_dwordx4 a[140:143], v43, %0 offset:640 nt" :  : "s"(Kb) : "memory","scc","v43","v42","s98","s99");
  asm volatile("s_waitcnt vmcnt(48)" :  :  : "memory");
  { u32 ln = LANE(), fa = lb + 11776u + (ln>>4)*288u, f3 = fa + 3456u;
  { u32x4 f = frag16(fa + 0u + (ln&15u)*2u, 36u); asm volatile("v_accvgpr_write_b32 a208, %0\n\tv_accvgpr_write_b32 a209, %1\n\tv_accvgpr_write_b32 a210, %2\n\tv_accvgpr_write_b32 a211, %3" :  : "v"(f[0]),"v"(f[1]),"v"(f[2]),"v"(f[3]) : "memory"); }
  { u32x4 f = frag16(fa + 1152u + (ln&15u)*2u, 36u); asm volatile("v_accvgpr_write_b32 a212, %0\n\tv_accvgpr_write_b32 a213, %1\n\tv_accvgpr_write_b32 a214, %2\n\tv_accvgpr_write_b32 a215, %3" :  : "v"(f[0]),"v"(f[1]),"v"(f[2]),"v"(f[3]) : "memory"); }
  { u32x4 f = frag16(fa + 2304u + (ln&15u)*2u, 36u); asm volatile("v_accvgpr_write_b32 a216, %0\n\tv_accvgpr_write_b32 a217, %1\n\tv_accvgpr_write_b32 a218, %2\n\tv_accvgpr_write_b32 a219, %3" :  : "v"(f[0]),"v"(f[1]),"v"(f[2]),"v"(f[3]) : "memory"); }
  { u32x4 f = frag16(f3 + (ln&15u)*2u, 36u); asm volatile("v_accvgpr_write_b32 a220, %0\n\tv_accvgpr_write_b32 a221, %1\n\tv_accvgpr_write_b32 a222, %2\n\tv_accvgpr_write_b32 a223, %3" :  : "v"(f[0]),"v"(f[1]),"v"(f[2]),"v"(f[3]) : "memory"); }
  { u32 t_ = (ln>>1)&3u; u32x4 f = frag16((t_ == 3u ? f3 : fa + t_*1152u) + 32u + (ln&1u)*2u, 36u); asm volatile("v_mov_b32 v52, %0\n\tv_mov_b32 v53, %1\n\tv_mov_b32 v54, %2\n\tv_mov_b32 v55, %3" :  : "v"(f[0]),"v"(f[1]),"v"(f[2]),"v"(f[3]) : "memory"); } }
  asm volatile("v_mov_b32 v40, 7264\n\ts_mov_b32 vcc_lo, 0x11111111\n\ts_mov_b32 vcc_hi, 0x11111111\n\tv_cndmask_b32 v4, v40, v47, vcc\n\ts_lshl_b64 vcc, vcc, 1\n\tv_cndmask_b32 v5, v40, v47, vcc\n\ts_lshl_b64 vcc, vcc, 1\n\tv_cndmask_b32 v6, v40, v47, vcc\n\ts_lshl_b64 vcc, vcc, 1\n\tv_cndmask_b32 v7, v40, v47, vcc\n\tds_read_b128 v[8:11], v4 offset:8704\n\tds_read_b128 v[12:15], v4 offset:8720\n\tds_read_b128 v[16:19], v5 offset:8704\n\tds_read_b128 v[20:23], v5 offset:8720\n\tds_read_b128 v[24:27], v6 offset:8704\n\tds_read_b128 v[28:31], v6 offset:8720\n\tds_read_b128 v[32:35], v7 offset:8704\n\tds_read_b128 v[36:39], v7 offset:8720\n\ts_waitcnt vmcnt(63)\n\tds_read_b128 a[240:243], v46 offset:16384\n\tds_read_b128 a[248:251], v46 offset:18432\n\tds_read_b128 a[244:247], v46 offset:17408\n\tds_read_b128 a[252:255], v46 offset:19456\n\ts_waitcnt lgkmcnt(4)\n\tv_mfma_f32_16x16x32_bf16 v[4:7], v[192:195], v[8:11], 0\n\tv_mfma_f32_16x16x32_bf16 v[40:43], v[200:203], v[8:11], 0\n\tv_mfma_f32_16x16x32_bf16 v[4:7], v[196:199], v[12:15], v[4:7]\n\tv_mfma_f32_16x16x32_bf16 v[40:43], v[204:207], v[12:15], v[40:43]\n\tv_mfma_f32_16x16x32_bf16 v[4:7], v[208:211], v[16:19], v[4:7]\n\tv_mfma_f32_16x16x32_bf16 v[40:43], v[216:219], v[16:19], v[40:43]\n\tv_mfma_f32_16x16x32_bf16 v[4:7], v[212:215], v[20:23], v[4:7]\n\tv_mfma_f32_16x16x32_bf16 v[40:43], v[220:223], v[20:23], v[40:43]\n\tv_mfma_f32_16x16x32_bf16 v[4:7], v[224:227], v[24:27], v[4:7]\n\tv_mfma_f32_16x16x32_bf16 v[40:43], v[232:235], v[24:27], v[40:43]\n\tv_mfma_f32_16x16x32_bf16 v[4:7], v[228:231], v[28:31], v[4:7]\n\tv_mfma_f32_16x16x32_bf16 v[40:43], v[236:239], v[28:31], v[40:43]\n\ts_waitcnt lgkmcnt(2)\n\tv_mfma_f32_16x16x32_bf16 v[4:7], a[240:243], v[32:35], v[4:7]\n\tv_mfma_f32_16x16x32_bf16 v[40:43], a[248:251], v[32:35], v[40:43]\n\ts_waitcnt lgkmcnt(0)\n\tv_mfma_f32_16x16x32_bf16 v[4:7], a[244:247], v[36:39], v[4:7]\n\tv_mfma_f32_16x16x32_bf16 v[40:43], a[252:255], v[36:39], v[40:43]\n\t1:\n\ts_memrealtime s[98:99]\n\ts_waitcnt lgkmcnt(0)\n\ts_sub_u32 s98, s98, %1\n\ts_cmp_lt_u32 s98, 1845\n\ts_cbranch_scc1 1b\n\tv_xor_b32 v14, 0x3c0000, v44\n\ts_add_u32 m0, %3, 8192\n\ts_nop 0\n\tglobal_load_lds_dwordx4 v14, %2 offset:0 nt\n\ts_add_u32 m0, %3, 9088\n\ts_nop 0\n\tglobal_load_lds_dwordx4 v14, %2 offset:128 nt\n\ts_add_u32 m0, %3, 9728\n\ts_nop 0\n\tglobal_load_lds_dwordx4 v14, %2 offset:512 nt\n\ts_add_u32 m0, %3, 10624\n\ts_nop 0\n\tglobal_load_lds_dwordx4 v14, %2 offset:640 nt\n\ts_nop 0\n\tv_add_f32_dpp v8, v5, v4 row_ror:8 row_mask:0xf bank_mask:0x3\n\tv_add_f32_dpp v9, v7, v6 row_ror:8 row_mask:0xf bank_mask:0x3\n\tv_add_f32_dpp v10, v41, v40 row_ror:8 row_mask:0xf bank_mask:0x3\n\tv_add_f32_dpp v11, v43, v42 row_ror:8 row_mask:0xf bank_mask:0x3\n\tv_add_f32_dpp v8, v4, v5 row_ror:8 row_mask:0xf bank_mask:0xc\n\tv_add_f32_dpp v9, v6, v7 row_ror:8 row_mask:0xf bank_mask:0xc\n\tv_add_f32_dpp v10, v40, v41 row_ror:8 row_mask:0xf bank_mask:0xc\n\tv_add_f32_dpp v11, v42, v43 row_ror:8 row_mask:0xf bank_mask:0xc\n\tv_max3_f32 v12, v8, v9, v10\n\tv_max3_f32 v12, v12, v11, v56\n\ts_nop 1\n\tv_max_f32_dpp v12, v12, v12 quad_perm:[1,0,3,2] row_mask:0xf bank_mask:0xf\n\ts_nop 1\n\tv_max_f32_dpp v12, v12, v12 quad_perm:[2,3,0,1] row_mask:0xf bank_mask:0xf\n\tv_mov_b32 v13, v12\n\ts_nop 1\n\tv_permlane16_swap_b32 v12, v13\n\ts_nop 0\n\tv_max_f32 v12, v12, v13\n\tv_mov_b32 v13, v12\n\ts_nop 1\n\tv_permlane32_swap_b32 v12, v13\n\ts_nop 0\n\tv_max_f32 v12, v12, v13\n\tv_mul_f32 v15, %0, v12\n\tv_fma_f32 v13, v56, %0, -v15\n\tv_fma_f32 v8, v8, %0, -v15\n\tv_fma_f32 v9, v9, %0, -v15\n\tv_fma_f32 v10, v10, %0, -v15\n\tv_fma_f32 v11, v11, %0, -v15\n\tv_exp_f32 v13, v13\n\tv_exp_f32 v8, v8\n\tv_exp_f32 v9, v9\n\tv_exp_f32 v10, v10\n\tv_exp_f32 v11, v11\n\tv_mov_b32 v56, v12\n\tv_cvt_pk_bf16_f32 v4, v8, v8\n\tv_cvt_pk_bf16_f32 v5, v9, v9\n\tv_cvt_pk_bf16_f32 v6, v10, v10\n\tv_cvt_pk_bf16_f32 v7, v11, v11\n\tv_add_f32 v8, v8, v9\n\tv_add_f32 v10, v10, v11\n\tv_fma_f32 v57, v57, v13, v8\n\tv_mul_f32 v48, v48, v13\n\tv_mul_f32 v49, v49, v13\n\tv_mul_f32 v50, v50, v13\n\tv_mul_f32 v51, v51, v13\n\tv_add_f32 v57, v57, v10\n\tv_and_b32_dpp v16, v4, v60 quad_perm:[0,0,0,0] row_mask:0xf bank_mask:0xf\n\tv_and_b32_dpp v17, v4, v61 quad_perm:[0,0,0,0] row_mask:0xf bank_mask:0xf\n\tv_and_b32_dpp v18, v4, v62 quad_perm:[0,0,0,0] row_mask:0xf bank_mask:0xf\n\tv_and_b32_dpp v19, v4, v63 quad_perm:[0,0,0,0] row_mask:0xf bank_mask:0xf\n\tv_and_b32_dpp v20, v5, v60 quad_perm:[0,0,0,0] row_mask:0xf bank_mask:0xf\n\tv_and_b32_dpp v21, v5, v61 quad_perm:[0,0,0,0] row_mask:0xf bank_mask:0xf\n\tv_and_b32_dpp v22, v5, v62 quad_perm:[0,0,0,0] row_mask:0xf bank_mask:0xf\n\tv_and_b32_dpp v23, v5, v63 quad_perm:[0,0,0,0] row_mask:0xf bank_mask:0xf\n\ts_waitcnt vmcnt(63)\n\tv_mfma_f32_16x16x32_bf16 v[48:51], a[0:3], v[16:19], v[48:51]\n\tv_and_b32_dpp v16, v6, v60 quad_perm:[0,0,0,0] row_mask:0xf bank_mask:0xf\n\tv_and_b32_dpp v17, v6, v61 quad_perm:[0,0,0,0] row_mask:0xf bank_mask:0xf\n\tv_and_b32_dpp v18, v6, v62 quad_perm:[0,0,0,0] row_mask:0xf bank_mask:0xf\n\tv_and_b32_dpp v19, v6, v63 quad_perm:[0,0,0,0] row_mask:0xf bank_mask:0xf\n\tv_mfma_f32_16x16x32_bf16 v[48:51], a[4:7], v[20:23], v[48:51]\n\tv_and_b32_dpp v20, v7, v60 quad_perm:[0,0,0,0] row_mask:0xf bank_mask:0xf\n\tv_and_b32_dpp v21, v7, v61 quad_perm:[0,0,0,0] row_mask:0xf bank_mask:0xf\n\tv_and_b32_dpp v22, v7, v62 quad_perm:[0,0,0,0] row_mask:0xf bank_mask:0xf\n\tv_and_b32_dpp v23, v7, v63 quad_perm:[0,0,0,0] row_mask:0xf bank_mask:0xf\n\tv_mfma_f32_16x16x32_bf16 v[48:51], a[8:11], v[16:19], v[48:51]\n\tv_and_b32_dpp v16, v4, v60 quad_perm:[1,1,1,1] row_mask:0xf bank_mask:0xf\n\tv_and_b32_dpp v17, v4, v61 quad_perm:[1,1,1,1] row_mask:0xf bank_mask:0xf\n\tv_and_b32_dpp v18, v4, v62 quad_perm:[1,1,1,1] row_mask:0xf bank_mask:0xf\n\tv_and_b32_dpp v19, v4, v63 quad_perm:[1,1,1,1] row_mask:0xf bank_mask:0xf\n\tv_mfma_f32_16x16x32_bf16 v[48:51], a[12:15], v[20:23], v[48:51]\n\tv_and_b32_dpp v20, v5, v60 quad_perm:[1,1,1,1] row_mask:0xf bank_mask:0xf\n\tv_and_b32_dpp v21, v5, v61 quad_perm:[1,1,1,1] row_mask:0xf bank_mask:0xf\n\tv_and_b32_dpp v22, v5, v62 quad_perm:[1,1,1,1] row_mask:0xf bank_mask:0xf\n\tv_and_b32_dpp v23, v5, v63 quad_perm:[1,1,1,1] row_mask:0xf bank_mask:0xf\n\ts_waitcnt vmcnt(63)\n\tv_mfma_f32_16x16x32_bf16 v[48:51], a[16:19], v[16:19], v[48:51]\n\tv_and_b32_dpp v16, v6, v60 quad_perm:[1,1,1,1] row_mask:0xf bank_mask:0xf\n\tv_and_b32_dpp v17, v6, v61 quad_perm:[1,1,1,1] row_mask:0xf bank_mask:0xf\n\tv_and_b32_dpp v18, v6, v62 quad_perm:[1,1,1,1] row_mask:0xf bank_mask:0xf\n\tv_and_b32_dpp v19, v6, v63 quad_perm:[1,1,1,1] row_mask:0xf bank_mask:0xf\n\tv_mfma_f32_16x16x32_bf16 v[48:51], a[20:23], v[20:23], v[48:51]\n\tv_and_b32_dpp v20, v7, v60 quad_perm:[1,1,1,1] row_mask:0xf bank_mask:0xf\n\tv_and_b32_dpp v21, v7, v61 quad_perm:[1,1,1,1] row_mask:0xf bank_mask:0xf\n\tv_and_b32_dpp v22, v7, v62 quad_perm:[1,1,1,1] row_mask:0xf bank_mask:0xf\n\tv_and_b32_dpp v23, v7, v63 quad_perm:[1,1,1,1] row_mask:0xf bank_mask:0xf\n\tv_mfma_f32_16x16x32_bf16 v[48:51], a[24:27], v[16:19], v[48:51]\n\tv_and_b32_dpp v16, v4, v60 quad_perm:[2,2,2,2] row_mask:0xf bank_mask:0xf\n\tv_and_b32_dpp v17, v4, v61 quad_perm:[2,2,2,2] row_mask:0xf bank_mask:0xf\n\tv_and_b32_dpp v18, v4, v62 quad_perm:[2,2,2,2] row_mask:0xf bank_mask:0xf\n\tv_and_b32_dpp v19, v4, v63 quad_perm:[2,2,2,2] row_mask:0xf bank_mask:0xf\n\tv_mfma_f32_16x16x32_bf16 v[48:51], a[28:31], v[20:23], v[48:51]\n\tv_and_b32_dpp v20, v5, v60 quad_perm:[2,2,2,2] row_mask:0xf bank_mask:0xf\n\tv_and_b32_dpp v21, v5, v61 quad_perm:[2,2,2,2] row_mask:0xf bank_mask:0xf\n\tv_and_b32_dpp v22, v5, v62 quad_perm:[2,2,2,2] row_mask:0xf bank_mask:0xf\n\tv_and_b32_dpp v23, v5, v63 quad_perm:[2,2,2,2] row_mask:0xf bank_mask:0xf\n\t1:\n\ts_memrealtime s[98:99]\n\ts_waitcnt lgkmcnt(0)\n\ts_sub_u32 s98, s98, %1\n\ts_cmp_lt_u32 s98, 1906\n\ts_cbranch_scc1 1b\n\tv_xor_b32 v14, 0x300000, v45\n\tglobal_load_dwordx4 a[144:147], v14, %4 offset:0 nt\n\tglobal_load_dwordx4 a[148:151], v14, %4 offset:256 nt\n\tglobal_load_dwordx4 a[152:155], v14, %4 offset:512 nt\n\tglobal_load_dwordx4 a[156:159], v14, %4 offset:768 nt\n\ts_waitcnt vmcnt(63)\n\tv_mfma_f32_16x16x32_bf16 v[48:51], a[32:35], v[16:19], v[48:51]\n\tv_and_b32_dpp v16, v6, v60 quad_perm:[2,2,2,2] row_mask:0xf bank_mask:0xf\n\tv_and_b32_dpp v17, v6, v61 quad_perm:[2,2,2,2] row_mask:0xf bank_mask:0xf\n\tv_and_b32_dpp v18, v6, v62 quad_perm:[2,2,2,2] row_mask:0xf bank_mask:0xf\n\tv_and_b32_dpp v19, v6, v63 quad_perm:[2,2,2,2] row_mask:0xf bank_mask:0xf\n\tv_mfma_f32_16x16x32_bf16 v[48:51], a[36:39], v[20:23], v[48:51]\n\tv_and_b32_dpp v20, v7, v60 quad_perm:[2,2,2,2] row_mask:0xf bank_mask:0xf\n\tv_and_b32_dpp v21, v7, v61 quad_perm:[2,2,2,2] row_mask:0xf bank_mask:0xf\n\tv_and_b32_dpp v22, v7, v62 quad_perm:[2,2,2,2] row_mask:0xf bank_mask:0xf\n\tv_and_b32_dpp v23, v7, v63 quad_perm:[2,2,2,2] row_mask:0xf bank_mask:0xf\n\tv_mfma_f32_16x16x32_bf16 v[48:51], a[40:43], v[16:19], v[48:51]\n\tv_and_b32_dpp v16, v4, v60 quad_perm:[3,3,3,3] row_mask:0xf bank_mask:0xf\n\tv_and_b32_dpp v17, v4, v61 quad_perm:[3,3,3,3] row_mask:0xf bank_mask:0xf\n\tv_and_b32_dpp v18, v4, v62 quad_perm:[3,3,3,3] row_mask:0xf bank_mask:0xf\n\tv_and_b32_dpp v19, v4, v63 quad_perm:[3,3,3,3] row_mask:0xf bank_mask:0xf\n\tv_mfma_f32_16x16x32_bf16 v[48:51], a[44:47], v[20:23], v[48:51]\n\tv_and_b32_dpp v20, v5, v60 quad_perm:[3,3,3,3] row_mask:0xf bank_mask:0xf\n\tv_and_b32_dpp v21, v5, v61 quad_perm:[3,3,3,3] row_mask:0xf bank_mask:0xf\n\tv_and_b32_dpp v22, v5, v62 quad_perm:[3,3,3,3] row_mask:0xf bank_mask:0xf\n\tv_and_b32_dpp v23, v5, v63 quad_perm:[3,3,3,3] row_mask:0xf bank_mask:0xf\n\ts_waitcnt vmcnt(57)\n\tv_mfma_f32_16x16x32_bf16 v[48:51], a[48:51], v[16:19], v[48:51]\n\tv_and_b32_dpp v16, v6, v60 quad_perm:[3,3,3,3] row_mask:0xf bank_mask:0xf\n\tv_and_b32_dpp v17, v6, v61 quad_perm:[3,3,3,3] row_mask:0xf bank_mask:0xf\n\tv_and_b32_dpp v18, v6, v62 quad_perm:[3,3,3,3] row_mask:0xf bank_mask:0xf\n\tv_and_b32_dpp v19, v6, v63 quad_perm:[3,3,3,3] row_mask:0xf bank_mask:0xf\n\tv_mfma_f32_16x16x32_bf16 v[48:51], a[52:55], v[20:23], v[48:51]\n\tv_and_b32_dpp v20, v7, v60 quad_perm:[3,3,3,3] row_mask:0xf bank_mask:0xf\n\tv_and_b32_dpp v21, v7, v61 quad_perm:[3,3,3,3] row_mask:0xf bank_mask:0xf\n\tv_and_b32_dpp v22, v7, v62 quad_perm:[3,3,3,3] row_mask:0xf bank_mask:0xf\n\tv_and_b32_dpp v23, v7, v63 quad_perm:[3,3,3,3] row_mask:0xf bank_mask:0xf\n\tv_mfma_f32_16x16x32_bf16 v[48:51], a[56:59], v[16:19], v[48:51]\n\ts_nop 0\n\tv_mfma_f32_16x16x32_bf16 v[48:51], a[60:63], v[20:23], v[48:51]" :  : "s"(CSC), "s"(tb), "s"(Kb), "s"(lb), "s"(Vb) : "memory","scc","vcc","s98","s99","v4","v5","v6","v7","v8","v9","v10","v11","v12","v13","v14","v15","v16","v17","v18","v19","v20","v21","v22","v23","v24","v25","v26","v27","v28","v29","v30","v31","v32","v33","v34","v35","v36","v37","v38","v39","v40","v41","v42","v43");
  { u32 to = w*4096u + LANE()*16u; asm volatile("global_load_dwordx4 v[240:243], %0, %1 offset:0 \n\tglobal_load_dwordx4 v[244:247], %0, %1 offset:1024 \n\tglobal_load_dwordx4 v[248:251], %0, %1 offset:2048 \n\tglobal_load_dwordx4 v[252:255], %0, %1 offset:3072 " :  : "v"(to), "s"(wsb + 36864ull) : "memory"); }
  pace(tb, 1968u);
  asm volatile("v_xor_b32 v43, 0x340000, v45\n\tglobal_load_dwordx4 a[160:163], v43, %0 offset:0 nt\n\tglobal_load_dwordx4 a[164:167], v43, %0 offset:256 nt\n\tglobal_load_dwordx4 a[168:171], v43, %0 offset:512 nt\n\tglobal_load_dwordx4 a[172:175], v43, %0 offset:768 nt" :  : "s"(Vb) : "memory","scc","v43","v42","s98","s99");
  asm volatile("v_mov_b32 v40, 7264\n\ts_mov_b32 vcc_lo, 0x11111111\n\ts_mov_b32 vcc_hi, 0x11111111\n\tv_cndmask_b32 v4, v40, v47, vcc\n\ts_lshl_b64 vcc, vcc, 1\n\tv_cndmask_b32 v5, v40, v47, vcc\n\ts_lshl_b64 vcc, vcc, 1\n\tv_cndmask_b32 v6, v40, v47, vcc\n\ts_lshl_b64 vcc, vcc, 1\n\tv_cndmask_b32 v7, v40, v47, vcc\n\tds_read_b128 v[8:11], v4 offset:8704\n\tds_read_b128 v[12:15], v4 offset:8720\n\tds_read_b128 v[16:19], v5 offset:8704\n\tds_read_b128 v[20:23], v5 offset:8720\n\tds_read_b128 v[24:27], v6 offset:8704\n\tds_read_b128 v[28:31], v6 offset:8720\n\tds_read_b128 v[32:35], v7 offset:8704\n\tds_read_b128 v[36:39], v7 offset:8720\n\ts_waitcnt vmcnt(44)\n\tds_read_b128 a[240:243], v46 offset:20480\n\tds_read_b128 a[248:251], v46 offset:22528\n\tds_read_b128 a[244:247], v46 offset:21504\n\tds_read_b128 a[252:255], v46 offset:23552\n\ts_waitcnt lgkmcnt(4)\n\ts_waitcnt lgkmcnt(2)\n\tv_mfma_f32_16x16x32_bf16 v[4:7], a[240:243], v[8:11], 0\n\tv_mfma_f32_16x16x32_bf16 v[40:43], a[248:251], v[8:11], 0\n\tds_read_b128 a[240:243], v46 offset:24576\n\tds_read_b128 a[248:251], v46 offset:26624\n\ts_waitcnt lgkmcnt(2)\n\tv_mfma_f32_16x16x32_bf16 v[4:7], a[244:247], v[12:15], v[4:7]\n\tv_mfma_f32_16x16x32_bf16 v[40:43], a[252:255], v[12:15], v[40:43]\n\tds_read_b128 a[244:247], v46 offset:25600\n\tds_read_b128 a[252:255], v46 offset:27648\n\ts_waitcnt lgkmcnt(2)\n\tv_mfma_f32_16x16x32_bf16 v[4:7], a[240:243], v[16:19], v[4:7]\n\tv_mfma_f32_16x16x32_bf16 v[40:43], a[248:251], v[16:19], v[40:43]\n\tds_read_b128 a[240:243], v46 offset:28672\n\tds_read_b128 a[248:251], v46 offset:30720\n\ts_waitcnt lgkmcnt(2)\n\tv_mfma_f32_16x16x32_bf16 v[4:7], a[244:247], v[20:23], v[4:7]\n\tv_mfma_f32_16x16x32_bf16 v[40:43], a[252:255], v[20:23], v[40:43]\n\tds_read_b128 a[244:247], v46 offset:29696\n\tds_read_b128 a[252:255], v46 offset:31744\n\ts_waitcnt lgkmcnt(2)\n\tv_mfma_f32_16x16x32_bf16 v[4:7], a[240:243], v[24:27], v[4:7]\n\tv_mfma_f32_16x16x32_bf16 v[40:43], a[248:251], v[24:27], v[40:43]\n\tds_read_b128 a[240:243], v46 offset:32768\n\tds_read_b128 a[248:251], v46 offset:34816\n\ts_waitcnt lgkmcnt(2)\n\tv_mfma_f32_16x16x32_bf16 v[4:7], a[244:247], v[28:31], v[4:7]\n\tv_mfma_f32_16x16x32_bf16 v[40:43], a[252:255], v[28:31], v[40:43]\n\tds_read_b128 a[244:247], v46 offset:33792\n\tds_read_b128 a[252:255], v46 offset:35840\n\ts_waitcnt lgkmcnt(2)\n\tv_mfma_f32_16x16x32_bf16 v[4:7], a[240:243], v[32:35], v[4:7]\n\tv_mfma_f32_16x16x32_bf16 v[40:43], a[248:251], v[32:35], v[40:43]\n\ts_waitcnt lgkmcnt(0)\n\tv_mfma_f32_16x16x32_bf16 v[4:7], a[244:247], v[36:39], v[4:7]\n\tv_mfma_f32_16x16x32_bf16 v[40:43], a[252:255], v[36:39], v[40:43]\n\t1:\n\ts_memrealtime s[98:99]\n\ts_waitcnt lgkmcnt(0)\n\ts_sub_u32 s98, s98, %1\n\ts_cmp_lt_u32 s98, 2030\n\ts_cbranch_scc1 1b\n\tv_xor_b32 v14, 0x380000, v45\n\tglobal_load_dwordx4 a[176:179], v14, %2 offset:0 nt\n\tglobal_load_dwordx4 a[180:183], v14, %2 offset:256 nt\n\tglobal_load_dwordx4 a[184:187], v14, %2 offset:512 nt\n\tglobal_load_dwordx4 a[188:191], v14, %2 offset:768 nt\n\ts_nop 0\n\tv_add_f32_dpp v8, v5, v4 row_ror:8 row_mask:0xf bank_mask:0x3\n\tv_add_f32_dpp v9, v7, v6 row_ror:8 row_mask:0xf bank_mask:0x3\n\tv_add_f32_dpp v10, v41, v40 row_ror:8 row_mask:0xf bank_mask:0x3\n\tv_add_f32_dpp v11, v43, v42 row_ror:8 row_mask:0xf bank_mask:0x3\n\tv_add_f32_dpp v8, v4, v5 row_ror:8 row_mask:0xf bank_mask:0xc\n\tv_add_f32_dpp v9, v6, v7 row_ror:8 row_mask:0xf bank_mask:0xc\n\tv_add_f32_dpp v10, v40, v41 row_ror:8 row_mask:0xf bank_mask:0xc\n\tv_add_f32_dpp v11, v42, v43 row_ror:8 row_mask:0xf bank_mask:0xc\n\tv_max3_f32 v12, v8, v9, v10\n\tv_max3_f32 v12, v12, v11, v56\n\ts_nop 1\n\tv_max_f32_dpp v12, v12, v12 quad_perm:[1,0,3,2] row_mask:0xf bank_mask:0xf\n\ts_nop 1\n\tv_max_f32_dpp v12, v12, v12 quad_perm:[2,3,0,1] row_mask:0xf bank_mask:0xf\n\tv_mov_b32 v13, v12\n\ts_nop 1\n\tv_permlane16_swap_b32 v12, v13\n\ts_nop 0\n\tv_max_f32 v12, v12, v13\n\tv_mov_b32 v13, v12\n\ts_nop 1\n\tv_permlane32_swap_b32 v12, v13\n\ts_nop 0\n\tv_max_f32 v12, v12, v13\n\tv_mul_f32 v15, %0, v12\n\tv_fma_f32 v13, v56, %0, -v15\n\tv_fma_f32 v8, v8, %0, -v15\n\tv_fma_f32 v9, v9, %0, -v15\n\tv_fma_f32 v10, v10, %0, -v15\n\tv_fma_f32 v11, v11, %0, -v15\n\tv_exp_f32 v13, v13\n\tv_exp_f32 v8, v8\n\tv_exp_f32 v9, v9\n\tv_exp_f32 v10, v10\n\tv_exp_f32 v11, v11\n\tv_mov_b32 v56, v12\n\tv_cvt_pk_bf16_f32 v4, v8, v8\n\tv_cvt_pk_bf16_f32 v5, v9, v9\n\tv_cvt_pk_bf16_f32 v6, v10, v10\n\tv_cvt_pk_bf16_f32 v7, v11, v11\n\tv_add_f32 v8, v8, v9\n\tv_add_f32 v10, v10, v11\n\tv_fma_f32 v57, v57, v13, v8\n\tv_mul_f32 v48, v48, v13\n\tv_mul_f32 v49, v49, v13\n\tv_mul_f32 v50, v50, v13\n\tv_mul_f32 v51, v51, v13\n\tv_add_f32 v57, v57, v10\n\tv_and_b32_dpp v16, v4, v60 quad_perm:[0,0,0,0] row_mask:0xf bank_mask:0xf\n\tv_and_b32_dpp v17, v4, v61 quad_perm:[0,0,0,0] row_mask:0xf bank_mask:0xf\n\tv_and_b32_dpp v18, v4, v62 quad_perm:[0,0,0,0] row_mask:0xf bank_mask:0xf\n\tv_and_b32_dpp v19, v4, v63 quad_perm:[0,0,0,0] row_mask:0xf bank_mask:0xf\n\tv_and_b32_dpp v20, v5, v60 quad_perm:[0,0,0,0] row_mask:0xf bank_mask:0xf\n\tv_and_b32_dpp v21, v5, v61 quad_perm:[0,0,0,0] row_mask:0xf bank_mask:0xf\n\tv_and_b32_dpp v22, v5, v62 quad_perm:[0,0,0,0] row_mask:0xf bank_mask:0xf\n\tv_and_b32_dpp v23, v5, v63 quad_perm:[0,0,0,0] row_mask:0xf bank_mask:0xf\n\ts_waitcnt vmcnt(44)\n\tv_mfma_f32_16x16x32_bf16 v[48:51], a[64:67], v[16:19], v[48:51]\n\tv_and_b32_dpp v16, v6, v60 quad_perm:[0,0,0,0] row_mask:0xf bank_mask:0xf\n\tv_and_b32_dpp v17, v6, v61 quad_perm:[0,0,0,0] row_mask:0xf bank_mask:0xf\n\tv_and_b32_dpp v18, v6, v62 quad_perm:[0,0,0,0] row_mask:0xf bank_mask:0xf\n\tv_and_b32_dpp v19, v6, v63 quad_perm:[0,0,0,0] row_mask:0xf bank_mask:0xf\n\tv_mfma_f32_16x16x32_bf16 v[48:51], a[68:71], v[20:23], v[48:51]\n\tv_and_b32_dpp v20, v7, v60 quad_perm:[0,0,0,0] row_mask:0xf bank_mask:0xf\n\tv_and_b32_dpp v21, v7, v61 quad_perm:[0,0,0,0] row_mask:0xf bank_mask:0xf\n\tv_and_b32_dpp v22, v7, v62 quad_perm:[0,0,0,0] row_mask:0xf bank_mask:0xf\n\tv_and_b32_dpp v23, v7, v63 quad_perm:[0,0,0,0] row_mask:0xf bank_mask:0xf\n\tv_mfma_f32_16x16x32_bf16 v[48:51], a[72:75], v[16:19], v[48:51]\n\tv_and_b32_dpp v16, v4, v60 quad_perm:[1,1,1,1] row_mask:0xf bank_mask:0xf\n\tv_and_b32_dpp v17, v4, v61 quad_perm:[1,1,1,1] row_mask:0xf bank_mask:0xf\n\tv_and_b32_dpp v18, v4, v62 quad_perm:[1,1,1,1] row_mask:0xf bank_mask:0xf\n\tv_and_b32_dpp v19, v4, v63 quad_perm:[1,1,1,1] row_mask:0xf bank_mask:0xf\n\tv_mfma_f32_16x16x32_bf16 v[48:51], a[76:79], v[20:23], v[48:51]\n\tv_and_b32_dpp v20, v5, v60 quad_perm:[1,1,1,1] row_mask:0xf bank_mask:0xf\n\tv_and_b32_dpp v21, v5, v61 quad_perm:[1,1,1,1] row_mask:0xf bank_mask:0xf\n\tv_and_b32_dpp v22, v5, v62 quad_perm:[1,1,1,1] row_mask:0xf bank_mask:0xf\n\tv_and_b32_dpp v23, v5, v63 quad_perm:[1,1,1,1] row_mask:0xf bank_mask:0xf\n\ts_waitcnt vmcnt(40)\n\tv_mfma_f32_16x16x32_bf16 v[48:51], a[80:83], v[16:19], v[48:51]\n\tv_and_b32_dpp v16, v6, v60 quad_perm:[1,1,1,1] row_mask:0xf bank_mask:0xf\n\tv_and_b32_dpp v17, v6, v61 quad_perm:[1,1,1,1] row_mask:0xf bank_mask:0xf\n\tv_and_b32_dpp v18, v6, v62 quad_perm:[1,1,1,1] row_mask:0xf bank_mask:0xf\n\tv_and_b32_dpp v19, v6, v63 quad_perm:[1,1,1,1] row_mask:0xf bank_mask:0xf\n\tv_mfma_f32_16x16x32_bf16 v[48:51], a[84:87], v[20:23], v[48:51]\n\tv_and_b32_dpp v20, v7, v60 quad_perm:[1,1,1,1] row_mask:0xf bank_mask:0xf\n\tv_and_b32_dpp v21, v7, v61 quad_perm:[1,1,1,1] row_mask:0xf bank_mask:0xf\n\tv_and_b32_dpp v22, v7, v62 quad_perm:[1,1,1,1] row_mask:0xf bank_mask:0xf\n\tv_and_b32_dpp v23, v7, v63 quad_perm:[1,1,1,1] row_mask:0xf bank_mask:0xf\n\tv_mfma_f32_16x16x32_bf16 v[48:51], a[88:91], v[16:19], v[48:51]\n\tv_and_b32_dpp v16, v4, v60 quad_perm:[2,2,2,2] row_mask:0xf bank_mask:0xf\n\tv_and_b32_dpp v17, v4, v61 quad_perm:[2,2,2,2] row_mask:0xf bank_mask:0xf\n\tv_and_b32_dpp v18, v4, v62 quad_perm:[2,2,2,2] row_mask:0xf bank_mask:0xf\n\tv_and_b32_dpp v19, v4, v63 quad_perm:[2,2,2,2] row_mask:0xf bank_mask:0xf\n\tv_mfma_f32_16x16x32_bf16 v[48:51], a[92:95], v[20:23], v[48:51]\n\tv_and_b32_dpp v20, v5, v60 quad_perm:[2,2,2,2] row_mask:0xf bank_mask:0xf\n\tv_and_b32_dpp v21, v5, v61 quad_perm:[2,2,2,2] row_mask:0xf bank_mask:0xf\n\tv_and_b32_dpp v22, v5, v62 quad_perm:[2,2,2,2] row_mask:0xf bank_mask:0xf\n\tv_and_b32_dpp v23, v5, v63 quad_perm:[2,2,2,2] row_mask:0xf bank_mask:0xf\n\ts_waitcnt vmcnt(36)\n\tv_mfma_f32_16x16x32_bf16 v[48:51], a[96:99], v[16:19], v[48:51]\n\tv_and_b32_dpp v16, v6, v60 quad_perm:[2,2,2,2] row_mask:0xf bank_mask:0xf\n\tv_and_b32_dpp v17, v6, v61 quad_perm:[2,2,2,2] row_mask:0xf bank_mask:0xf\n\tv_and_b32_dpp v18, v6, v62 quad_perm:[2,2,2,2] row_mask:0xf bank_mask:0xf\n\tv_and_b32_dpp v19, v6, v63 quad_perm:[2,2,2,2] row_mask:0xf bank_mask:0xf\n\tv_mfma_f32_16x16x32_bf16 v[48:51], a[100:103], v[20:23], v[48:51]\n\tv_and_b32_dpp v20, v7, v60 quad_perm:[2,2,2,2] row_mask:0xf bank_mask:0xf\n\tv_and_b32_dpp v21, v7, v61 quad_perm:[2,2,2,2] row_mask:0xf bank_mask:0xf\n\tv_and_b32_dpp v22, v7, v62 quad_perm:[2,2,2,2] row_mask:0xf bank_mask:0xf\n\tv_and_b32_dpp v23, v7, v63 quad_perm:[2,2,2,2] row_mask:0xf bank_mask:0xf\n\tv_mfma_f32_16x16x32_bf16 v[48:51], a[104:107], v[16:19], v[48:51]\n\tv_and_b32_dpp v16, v4, v60 quad_perm:[3,3,3,3] row_mask:0xf bank_mask:0xf\n\tv_and_b32_dpp v17, v4, v61 quad_perm:[3,3,3,3] row_mask:0xf bank_mask:0xf\n\tv_and_b32_dpp v18, v4, v62 quad_perm:[3,3,3,3] row_mask:0xf bank_mask:0xf\n\tv_and_b32_dpp v19, v4, v63 quad_perm:[3,3,3,3] row_mask:0xf bank_mask:0xf\n\tv_mfma_f32_16x16x32_bf16 v[48:51], a[108:111], v[20:23], v[48:51]\n\tv_and_b32_dpp v20, v5, v60 quad_perm:[3,3,3,3] row_mask:0xf bank_mask:0xf\n\tv_and_b32_dpp v21, v5, v61 quad_perm:[3,3,3,3] row_mask:0xf bank_mask:0xf\n\tv_and_b32_dpp v22, v5, v62 quad_perm:[3,3,3,3] row_mask:0xf bank_mask:0xf\n\tv_and_b32_dpp v23, v5, v63 quad_perm:[3,3,3,3] row_mask:0xf bank_mask:0xf\n\ts_waitcnt vmcnt(32)\n\tv_mfma_f32_16x16x32_bf16 v[48:51], a[112:115], v[16:19], v[48:51]\n\tv_and_b32_dpp v16, v6, v60 quad_perm:[3,3,3,3] row_mask:0xf bank_mask:0xf\n\tv_and_b32_dpp v17, v6, v61 quad_perm:[3,3,3,3] row_mask:0xf bank_mask:0xf\n\tv_and_b32_dpp v18, v6, v62 quad_perm:[3,3,3,3] row_mask:0xf bank_mask:0xf\n\tv_and_b32_dpp v19, v6, v63 quad_perm:[3,3,3,3] row_mask:0xf bank_mask:0xf\n\tv_mfma_f32_16x16x32_bf16 v[48:51], a[116:119], v[20:23], v[48:51]\n\tv_and_b32_dpp v20, v7, v60 quad_perm:[3,3,3,3] row_mask:0xf bank_mask:0xf\n\tv_and_b32_dpp v21, v7, v61 quad_perm:[3,3,3,3] row_mask:0xf bank_mask:0xf\n\tv_and_b32_dpp v22, v7, v62 quad_perm:[3,3,3,3] row_mask:0xf bank_mask:0xf\n\tv_and_b32_dpp v23, v7, v63 quad_perm:[3,3,3,3] row_mask:0xf bank_mask:0xf\n\tv_mfma_f32_16x16x32_bf16 v[48:51], a[120:123], v[16:19], v[48:51]\n\ts_nop 0\n\tv_mfma_f32_16x16x32_bf16 v[48:51], a[124:127], v[20:23], v[48:51]\n\t1:\n\ts_memrealtime s[98:99]\n\ts_waitcnt lgkmcnt(0)\n\ts_sub_u32 s98, s98, %1\n\ts_cmp_lt_u32 s98, 2091\n\ts_cbranch_scc1 1b\n\tv_xor_b32 v14, 0x3c0000, v45\n\tglobal_load_dwordx4 a[192:195], v14, %2 offset:0 nt\n\tglobal_load_dwordx4 a[196:199], v14, %2 offset:256 nt\n\tglobal_load_dwordx4 a[200:203], v14, %2 offset:512 nt\n\tglobal_load_dwordx4 a[204:207], v14, %2 offset:768 nt" :  : "s"(CSC), "s"(tb), "s"(Vb) : "memory","scc","vcc","s98","s99","v4","v5","v6","v7","v8","v9","v10","v11","v12","v13","v14","v15","v16","v17","v18","v19","v20","v21","v22","v23","v24","v25","v26","v27","v28","v29","v30","v31","v32","v33","v34","v35","v36","v37","v38","v39","v40","v41","v42","v43");
  { u32 ln = LANE(); u32 to = w*4096u + ln*16u; u64 tb_ = wsb + 36864ull; u32x4 a0,a1,a2,a3;
  asm volatile("s_waitcnt vmcnt(12)\n\tv_mov_b32 %0, v240\n\tv_mov_b32 %1, v241\n\tv_mov_b32 %2, v242\n\tv_mov_b32 %3, v243\n\tv_mov_b32 %4, v244\n\tv_mov_b32 %5, v245\n\tv_mov_b32 %6, v246\n\tv_mov_b32 %7, v247\n\tv_mov_b32 %8, v248\n\tv_mov_b32 %9, v249\n\tv_mov_b32 %10, v250\n\tv_mov_b32 %11, v251\n\tv_mov_b32 %12, v252\n\tv_mov_b32 %13, v253\n\tv_mov_b32 %14, v254\n\tv_mov_b32 %15, v255" : "=v"(a0[0]),"=v"(a0[1]),"=v"(a0[2]),"=v"(a0[3]),"=v"(a1[0]),"=v"(a1[1]),"=v"(a1[2]),"=v"(a1[3]),"=v"(a2[0]),"=v"(a2[1]),"=v"(a2[2]),"=v"(a2[3]),"=v"(a3[0]),"=v"(a3[1]),"=v"(a3[2]),"=v"(a3[3]) :  : "memory");
  _Pragma("unroll 1") for (int sp = 0; sp < SPIN; sp++) { bool ok = tagok(a0,tag) && tagok(a1,tag) && tagok(a2,tag) && tagok(a3,tag); if (__all(ok)) break; __builtin_amdgcn_s_sleep(1);
  asm volatile("global_load_dwordx4 %0, %4, %5 offset:0 sc1\n\tglobal_load_dwordx4 %1, %4, %5 offset:1024 sc1\n\tglobal_load_dwordx4 %2, %4, %5 offset:2048 sc1\n\tglobal_load_dwordx4 %3, %4, %5 offset:3072 sc1\n\ts_waitcnt vmcnt(0)" : "=&v"(a0),"=&v"(a1),"=&v"(a2),"=&v"(a3) : "v"(to), "s"(tb_) : "memory");
  }
  u32 ta = 0u + (to>>1); *LDSP(u32x2, ta) = u32x2{a0[0],a0[2]}; *LDSP(u32x2, ta+512) = u32x2{a1[0],a1[2]}; *LDSP(u32x2, ta+1024) = u32x2{a2[0],a2[2]}; *LDSP(u32x2, ta+1536) = u32x2{a3[0],a3[2]}; }
  asm volatile("v_mov_b32 v40, 7264\n\ts_mov_b32 vcc_lo, 0x11111111\n\ts_mov_b32 vcc_hi, 0x11111111\n\tv_cndmask_b32 v4, v40, v47, vcc\n\ts_lshl_b64 vcc, vcc, 1\n\tv_cndmask_b32 v5, v40, v47, vcc\n\ts_lshl_b64 vcc, vcc, 1\n\tv_cndmask_b32 v6, v40, v47, vcc\n\ts_lshl_b64 vcc, vcc, 1\n\tv_cndmask_b32 v7, v40, v47, vcc\n\tds_read_b128 v[24:27], v6 offset:8704\n\tds_read_b128 v[28:31], v6 offset:8720\n\tds_read_b128 v[8:11], v4 offset:8704\n\tds_read_b128 v[12:15], v4 offset:8720\n\tds_read_b128 v[16:19], v5 offset:8704\n\tds_read_b128 v[20:23], v5 offset:8720\n\tds_read_b128 v[32:35], v7 offset:8704\n\tds_read_b128 v[36:39], v7 offset:8720\n\ts_waitcnt vmcnt(20)\n\tds_read_b128 a[240:243], v46 offset:0\n\tds_read_b128 a[248:251], v46 offset:2048\n\tds_read_b128 a[244:247], v46 offset:1024\n\tds_read_b128 a[252:255], v46 offset:3072\n\ts_waitcnt lgkmcnt(4)\n\tv_mfma_f32_16x16x32_bf16 v[4:7], a[128:131], v[24:27], 0\n\tv_mfma_f32_16x16x32_bf16 v[40:43], a[136:139], v[24:27], 0\n\tv_mfma_f32_16x16x32_bf16 v[4:7], a[132:135], v[28:31], v[4:7]\n\tv_mfma_f32_16x16x32_bf16 v[40:43], a[140:143], v[28:31], v[40:43]\n\ts_waitcnt lgkmcnt(2)\n\tv_mfma_f32_16x16x32_bf16 v[4:7], a[240:243], v[8:11], v[4:7]\n\tv_mfma_f32_16x16x32_bf16 v[40:43], a[248:251], v[8:11], v[40:43]\n\tds_read_b128 a[240:243], v46 offset:4096\n\tds_read_b128 a[248:251], v46 offset:6144\n\ts_waitcnt lgkmcnt(2)\n\tv_mfma_f32_16x16x32_bf16 v[4:7], a[244:247], v[12:15], v[4:7]\n\tv_mfma_f32_16x16x32_bf16 v[40:43], a[252:255], v[12:15], v[40:43]\n\tds_read_b128 a[244:247], v46 offset:5120\n\tds_read_b128 a[252:255], v46 offset:7168\n\ts_waitcnt lgkmcnt(2)\n\tv_mfma_f32_16x16x32_bf16 v[4:7], a[240:243], v[16:19], v[4:7]\n\tv_mfma_f32_16x16x32_bf16 v[40:43], a[248:251], v[16:19], v[40:43]\n\tds_read_b128 a[240:243], v46 offset:8192\n\tds_read_b128 a[248:251], v46 offset:10240\n\ts_waitcnt lgkmcnt(2)\n\tv_mfma_f32_16x16x32_bf16 v[4:7], a[244:247], v[20:23], v[4:7]\n\tv_mfma_f32_16x16x32_bf16 v[40:43], a[252:255], v[20:23], v[40:43]\n\tds_read_b128 a[244:247], v46 offset:9216\n\tds_read_b128 a[252:255], v46 offset:11264\n\ts_waitcnt lgkmcnt(2)\n\tv_mfma_f32_16x16x32_bf16 v[4:7], a[240:243], v[32:35], v[4:7]\n\tv_mfma_f32_16x16x32_bf16 v[40:43], a[248:251], v[32:35], v[40:43]\n\ts_waitcnt lgkmcnt(0)\n\tv_mfma_f32_16x16x32_bf16 v[4:7], a[244:247], v[36:39], v[4:7]\n\tv_mfma_f32_16x16x32_bf16 v[40:43], a[252:255], v[36:39], v[40:43]\n\ts_nop 7\n\ts_nop 0\n\tv_add_f32_dpp v8, v5, v4 row_ror:8 row_mask:0xf bank_mask:0x3\n\tv_add_f32_dpp v9, v7, v6 row_ror:8 row_mask:0xf bank_mask:0x3\n\tv_add_f32_dpp v10, v41, v40 row_ror:8 row_mask:0xf bank_mask:0x3\n\tv_add_f32_dpp v11, v43, v42 row_ror:8 row_mask:0xf bank_mask:0x3\n\tv_add_f32_dpp v8, v4, v5 row_ror:8 row_mask:0xf bank_mask:0xc\n\tv_add_f32_dpp v9, v6, v7 row_ror:8 row_mask:0xf bank_mask:0xc\n\tv_add_f32_dpp v10, v40, v41 row_ror:8 row_mask:0xf bank_mask:0xc\n\tv_add_f32_dpp v11, v42, v43 row_ror:8 row_mask:0xf bank_mask:0xc\n\tv_max3_f32 v12, v8, v9, v10\n\tv_max3_f32 v12, v12, v11, v56\n\ts_nop 1\n\tv_max_f32_dpp v12, v12, v12 quad_perm:[1,0,3,2] row_mask:0xf bank_mask:0xf\n\ts_nop 1\n\tv_max_f32_dpp v12, v12, v12 quad_perm:[2,3,0,1] row_mask:0xf bank_mask:0xf\n\tv_mov_b32 v13, v12\n\ts_nop 1\n\tv_permlane16_swap_b32 v12, v13\n\ts_nop 0\n\tv_max_f32 v12, v12, v13\n\tv_mov_b32 v13, v12\n\ts_nop 1\n\tv_permlane32_swap_b32 v12, v13\n\ts_nop 0\n\tv_max_f32 v12, v12, v13\n\tv_mul_f32 v15, %0, v12\n\tv_fma_f32 v13, v56, %0, -v15\n\tv_fma_f32 v8, v8, %0, -v15\n\tv_fma_f32 v9, v9, %0, -v15\n\tv_fma_f32 v10, v10, %0, -v15\n\tv_fma_f32 v11, v11, %0, -v15\n\tv_exp_f32 v13, v13\n\tv_exp_f32 v8, v8\n\tv_exp_f32 v9, v9\n\tv_exp_f32 v10, v10\n\tv_exp_f32 v11, v11\n\tv_mov_b32 v56, v12\n\tv_cvt_pk_bf16_f32 v4, v8, v8\n\tv_cvt_pk_bf16_f32 v5, v9, v9\n\tv_cvt_pk_bf16_f32 v6, v10, v10\n\tv_cvt_pk_bf16_f32 v7, v11, v11\n\tv_add_f32 v8, v8, v9\n\tv_add_f32 v10, v10, v11\n\tv_fma_f32 v57, v57, v13, v8\n\tv_mul_f32 v48, v48, v13\n\tv_mul_f32 v49, v49, v13\n\tv_mul_f32 v50, v50, v13\n\tv_mul_f32 v51, v51, v13\n\tv_add_f32 v57, v57, v10\n\tv_and_b32_dpp v16, v4, v60 quad_perm:[0,0,0,0] row_mask:0xf bank_mask:0xf\n\tv_and_b32_dpp v17, v4, v61 quad_perm:[0,0,0,0] row_mask:0xf bank_mask:0xf\n\tv_and_b32_dpp v18, v4, v62 quad_perm:[0,0,0,0] row_mask:0xf bank_mask:0xf\n\tv_and_b32_dpp v19, v4, v63 quad_perm:[0,0,0,0] row_mask:0xf bank_mask:0xf\n\tv_and_b32_dpp v20, v5, v60 quad_perm:[0,0,0,0] row_mask:0xf bank_mask:0xf\n\tv_and_b32_dpp v21, v5, v61 quad_perm:[0,0,0,0] row_mask:0xf bank_mask:0xf\n\tv_and_b32_dpp v22, v5, v62 quad_perm:[0,0,0,0] row_mask:0xf bank_mask:0xf\n\tv_and_b32_dpp v23, v5, v63 quad_perm:[0,0,0,0] row_mask:0xf bank_mask:0xf\n\t1:\n\ts_memrealtime s[98:99]\n\ts_waitcnt lgkmcnt(0)\n\ts_sub_u32 s98, s98, %1\n\ts_cmp_lt_u32 s98, 2152\n\ts_cbranch_scc1 1b\n\tv_xor_b32 v14, 0x4000000, v44\n\ts_add_u32 m0, %3, 12288\n\ts_nop 0\n\tglobal_load_lds_dwordx4 v14, %2 offset:0 nt\n\ts_add_u32 m0, %3, 13184\n\ts_nop 0\n\tglobal_load_lds_dwordx4 v14, %2 offset:128 nt\n\ts_add_u32 m0, %3, 13824\n\ts_nop 0\n\tglobal_load_lds_dwordx4 v14, %2 offset:512 nt\n\ts_add_u32 m0, %3, 14720\n\ts_nop 0\n\tglobal_load_lds_dwordx4 v14, %2 offset:640 nt\n\ts_waitcnt vmcnt(20)\n\tv_mfma_f32_16x16x32_bf16 v[48:51], a[144:147], v[16:19], v[48:51]\n\tv_and_b32_dpp v16, v6, v60 quad_perm:[0,0,0,0] row_mask:0xf bank_mask:0xf\n\tv_and_b32_dpp v17, v6, v61 quad_perm:[0,0,0,0] row_mask:0xf bank_mask:0xf\n\tv_and_b32_dpp v18, v6, v62 quad_perm:[0,0,0,0] row_mask:0xf bank_mask:0xf\n\tv_and_b32_dpp v19, v6, v63 quad_perm:[0,0,0,0] row_mask:0xf bank_mask:0xf\n\tv_mfma_f32_16x16x32_bf16 v[48:51], a[148:151], v[20:23], v[48:51]\n\tv_and_b32_dpp v20, v7, v60 quad_perm:[0,0,0,0] row_mask:0xf bank_mask:0xf\n\tv_and_b32_dpp v21, v7, v61 quad_perm:[0,0,0,0] row_mask:0xf bank_mask:0xf\n\tv_and_b32_dpp v22, v7, v62 quad_perm:[0,0,0,0] row_mask:0xf bank_mask:0xf\n\tv_and_b32_dpp v23, v7, v63 quad_perm:[0,0,0,0] row_mask:0xf bank_mask:0xf\n\tv_mfma_f32_16x16x32_bf16 v[48:51], a[152:155], v[16:19], v[48:51]\n\tv_and_b32_dpp v16, v4, v60 quad_perm:[1,1,1,1] row_mask:0xf bank_mask:0xf\n\tv_and_b32_dpp v17, v4, v61 quad_perm:[1,1,1,1] row_mask:0xf bank_mask:0xf\n\tv_and_b32_dpp v18, v4, v62 quad_perm:[1,1,1,1] row_mask:0xf bank_mask:0xf\n\tv_and_b32_dpp v19, v4, v63 quad_perm:[1,1,1,1] row_mask:0xf bank_mask:0xf\n\tv_mfma_f32_16x16x32_bf16 v[48:51], a[156:159], v[20:23], v[48:51]\n\tv_and_b32_dpp v20, v5, v60 quad_perm:[1,1,1,1] row_mask:0xf bank_mask:0xf\n\tv_and_b32_dpp v21, v5, v61 quad_perm:[1,1,1,1] row_mask:0xf bank_mask:0xf\n\tv_and_b32_dpp v22, v5, v62 quad_perm:[1,1,1,1] row_mask:0xf bank_mask:0xf\n\tv_and_b32_dpp v23, v5, v63 quad_perm:[1,1,1,1] row_mask:0xf bank_mask:0xf\n\ts_waitcnt vmcnt(12)\n\tv_mfma_f32_16x16x32_bf16 v[48:51], a[160:163], v[16:19], v[48:51]\n\tv_and_b32_dpp v16, v6, v60 quad_perm:[1,1,1,1] row_mask:0xf bank_mask:0xf\n\tv_and_b32_dpp v17, v6, v61 quad_perm:[1,1,1,1] row_mask:0xf bank_mask:0xf\n\tv_and_b32_dpp v18, v6, v62 quad_perm:[1,1,1,1] row_mask:0xf bank_mask:0xf\n\tv_and_b32_dpp v19, v6, v63 quad_perm:[1,1,1,1] row_mask:0xf bank_mask:0xf\n\tv_mfma_f32_16x16x32_bf16 v[48:51], a[164:167], v[20:23], v[48:51]\n\tv_and_b32_dpp v20, v7, v60 quad_perm:[1,1,1,1] row_mask:0xf bank_mask:0xf\n\tv_and_b32_dpp v21, v7, v61 quad_perm:[1,1,1,1] row_mask:0xf bank_mask:0xf\n\tv_and_b32_dpp v22, v7, v62 quad_perm:[1,1,1,1] row_mask:0xf bank_mask:0xf\n\tv_and_b32_dpp v23, v7, v63 quad_perm:[1,1,1,1] row_mask:0xf bank_mask:0xf\n\tv_mfma_f32_16x16x32_bf16 v[48:51], a[168:171], v[16:19], v[48:51]\n\tv_and_b32_dpp v16, v4, v60 quad_perm:[2,2,2,2] row_mask:0xf bank_mask:0xf\n\tv_and_b32_dpp v17, v4, v61 quad_perm:[2,2,2,2] row_mask:0xf bank_mask:0xf\n\tv_and_b32_dpp v18, v4, v62 quad_perm:[2,2,2,2] row_mask:0xf bank_mask:0xf\n\tv_and_b32_dpp v19, v4, v63 quad_perm:[2,2,2,2] row_mask:0xf bank_mask:0xf\n\tv_mfma_f32_16x16x32_bf16 v[48:51], a[172:175], v[20:23], v[48:51]\n\tv_and_b32_dpp v20, v5, v60 quad_perm:[2,2,2,2] row_mask:0xf bank_mask:0xf\n\tv_and_b32_dpp v21, v5, v61 quad_perm:[2,2,2,2] row_mask:0xf bank_mask:0xf\n\tv_and_b32_dpp v22, v5, v62 quad_perm:[2,2,2,2] row_mask:0xf bank_mask:0xf\n\tv_and_b32_dpp v23, v5, v63 quad_perm:[2,2,2,2] row_mask:0xf bank_mask:0xf\n\ts_waitcnt vmcnt(8)\n\tv_mfma_f32_16x16x32_bf16 v[48:51], a[176:179], v[16:19], v[48:51]\n\tv_and_b32_dpp v16, v6, v60 quad_perm:[2,2,2,2] row_mask:0xf bank_mask:0xf\n\tv_and_b32_dpp v17, v6, v61 quad_perm:[2,2,2,2] row_mask:0xf bank_mask:0xf\n\tv_and_b32_dpp v18, v6, v62 quad_perm:[2,2,2,2] row_mask:0xf bank_mask:0xf\n\tv_and_b32_dpp v19, v6, v63 quad_perm:[2,2,2,2] row_mask:0xf bank_mask:0xf\n\tv_mfma_f32_16x16x32_bf16 v[48:51], a[180:183], v[20:23], v[48:51]\n\tv_and_b32_dpp v20, v7, v60 quad_perm:[2,2,2,2] row_mask:0xf bank_mask:0xf\n\tv_and_b32_dpp v21, v7, v61 quad_perm:[2,2,2,2] row_mask:0xf bank_mask:0xf\n\tv_and_b32_dpp v22, v7, v62 quad_perm:[2,2,2,2] row_mask:0xf bank_mask:0xf\n\tv_and_b32_dpp v23, v7, v63 quad_perm:[2,2,2,2] row_mask:0xf bank_mask:0xf\n\tv_mfma_f32_16x16x32_bf16 v[48:51], a[184:187], v[16:19], v[48:51]\n\tv_and_b32_dpp v16, v4, v60 quad_perm:[3,3,3,3] row_mask:0xf bank_mask:0xf\n\tv_and_b32_dpp v17, v4, v61 quad_perm:[3,3,3,3] row_mask:0xf bank_mask:0xf\n\tv_and_b32_dpp v18, v4, v62 quad_perm:[3,3,3,3] row_mask:0xf bank_mask:0xf\n\tv_and_b32_dpp v19, v4, v63 quad_perm:[3,3,3,3] row_mask:0xf bank_mask:0xf\n\tv_mfma_f32_16x16x32_bf16 v[48:51], a[188:191], v[20:23], v[48:51]\n\tv_and_b32_dpp v20, v5, v60 quad_perm:[3,3,3,3] row_mask:0xf bank_mask:0xf\n\tv_and_b32_dpp v21, v5, v61 quad_perm:[3,3,3,3] row_mask:0xf bank_mask:0xf\n\tv_and_b32_dpp v22, v5, v62 quad_perm:[3,3,3,3] row_mask:0xf bank_mask:0xf\n\tv_and_b32_dpp v23, v5, v63 quad_perm:[3,3,3,3] row_mask:0xf bank_mask:0xf\n\tv_mbcnt_lo_u32_b32 v40, -1, 0\n\tv_mbcnt_hi_u32_b32 v40, -1, v40\n\tv_add_f32_dpp v41, v57, v57 quad_perm:[1,0,3,2] row_mask:0xf bank_mask:0xf\n\tv_and_b32 v42, 4, v40\n\tv_lshrrev_b32 v43, 4, v40\n\tv_add_f32_dpp v41, v41, v41 quad_perm:[2,3,0,1] row_mask:0xf bank_mask:0xf\n\tv_add_u32 v42, v42, v43\n\tv_mov_b32 v43, v41\n\tv_lshl_add_u32 v13, v42, 7, %4\n\tv_and_b32 v42, 8, v40\n\tv_permlane16_swap_b32 v41, v43\n\tv_lshl_add_u32 v13, v42, 1, v13\n\tv_add_f32 v41, v41, v43\n\tv_and_b32 v42, 3, v40\n\tv_mov_b32 v43, v41\n\tv_lshl_add_u32 v13, v42, 2, v13\n\tv_lshl_add_u32 v12, v40, 3, %5\n\tv_permlane32_swap_b32 v41, v43\n\tv_mul_f32 v40, %0, v56\n\tv_add_f32 v41, v41, v43\n\ts_mov_b64 exec, 0x11\n\tds_write_b64 v12, v[40:41]\n\ts_mov_b64 exec, -1\n\ts_waitcnt vmcnt(4)\n\tv_mfma_f32_16x16x32_bf16 v[48:51], a[192:195], v[16:19], v[48:51]\n\tv_and_b32_dpp v16, v6, v60 quad_perm:[3,3,3,3] row_mask:0xf bank_mask:0xf\n\tv_and_b32_dpp v17, v6, v61 quad_perm:[3,3,3,3] row_mask:0xf bank_mask:0xf\n\tv_and_b32_dpp v18, v6, v62 quad_perm:[3,3,3,3] row_mask:0xf bank_mask:0xf\n\tv_and_b32_dpp v19, v6, v63 quad_perm:[3,3,3,3] row_mask:0xf bank_mask:0xf\n\tv_mfma_f32_16x16x32_bf16 v[48:51], a[196:199], v[20:23], v[48:51]\n\tv_and_b32_dpp v20, v7, v60 quad_perm:[3,3,3,3] row_mask:0xf bank_mask:0xf\n\tv_and_b32_dpp v21, v7, v61 quad_perm:[3,3,3,3] row_mask:0xf bank_mask:0xf\n\tv_and_b32_dpp v22, v7, v62 quad_perm:[3,3,3,3] row_mask:0xf bank_mask:0xf\n\tv_and_b32_dpp v23, v7, v63 quad_perm:[3,3,3,3] row_mask:0xf bank_mask:0xf\n\tv_mfma_f32_16x16x32_bf16 v[48:51], a[200:203], v[16:19], v[48:51]\n\ts_nop 0\n\tv_mfma_f32_16x16x32_bf16 v[48:51], a[204:207], v[20:23], v[48:51]\n\ts_nop 7\n\ts_nop 0\n\tds_write2_b32 v13, v48, v49 offset1:8\n\tds_write2_b32 v13, v50, v51 offset0:16 offset1:24" :  : "s"(CSC), "s"(tb), "s"(Kb), "s"(lb), "s"(MRG + w*1024u), "s"(MLB + w*8u) : "memory","scc","vcc","s98","s99","v4","v5","v6","v7","v8","v9","v10","v11","v12","v13","v14","v15","v16","v17","v18","v19","v20","v21","v22","v23","v24","v25","v26","v27","v28","v29","v30","v31","v32","v33","v34","v35","v36","v37","v38","v39","v40","v41","v42","v43");
  tref = merge_publish(w, s, wsb + 65536ull + (u64)kvh*10240u);
  if (wg == 0 && w == 1 && LANE() == 0) { u32 z = 0; asm volatile("global_store_dword %0, %1, %2 sc1" :  : "v"(z), "v"(tag), "s"(wsb) : "memory"); }
  {
  u32 ln = LANE(); u32 po = (2*xg + (w>>1))*10240u + (w&1)*4096u + gpos(ln>>4, ln&15u); u32 pm = (2*xg + (w>>1))*10240u + 8192u + (ln&15u)*16u + (w&1)*8u;
  u64 pb = wsb + 65536ull; u32x4 a0,a1,a2,a3; u32x2 ml;
  u32 bpa = ln & 48u, rda = RED + ln*16u - (ln>>5)*256u + w*4u; u64 yd = pin(wsb + 720896ull + (u64)(xg*2560 + i32*80));
  asm volatile("" : "+v"(po), "+v"(pm), "+v"(bpa), "+v"(rda));
  pace(tref, 60);
  asm volatile("global_load_dwordx4 %0, %5, %7 offset:0 \n\tglobal_load_dwordx4 %1, %5, %7 offset:1024 \n\tglobal_load_dwordx4 %2, %5, %7 offset:2048 \n\tglobal_load_dwordx4 %3, %5, %7 offset:3072 \n\tglobal_load_dwordx2 %4, %6, %7 \n\ts_waitcnt vmcnt(0)" : "=&v"(a0),"=&v"(a1),"=&v"(a2),"=&v"(a3),"=&v"(ml) : "v"(po), "v"(pm), "s"(pb) : "memory","scc","v43","v42","s98","s99");
  _Pragma("unroll 1") for (int sp = 0; sp < SPIN; sp++) { u32 mx = umax3(umax4(a0), umax4(a1), umax3(umax4(a2), umax4(a3), max(ml[0], ml[1]))); if (!__any(mx == 0xFFFFFFFFu)) break; __builtin_amdgcn_s_sleep(1);
  asm volatile("global_load_dwordx4 %0, %5, %7 offset:0 sc1\n\tglobal_load_dwordx4 %1, %5, %7 offset:1024 sc1\n\tglobal_load_dwordx4 %2, %5, %7 offset:2048 sc1\n\tglobal_load_dwordx4 %3, %5, %7 offset:3072 sc1\n\tglobal_load_dwordx2 %4, %6, %7 sc1\n\ts_waitcnt vmcnt(0)" : "=&v"(a0),"=&v"(a1),"=&v"(a2),"=&v"(a3),"=&v"(ml) : "v"(po), "v"(pm), "s"(pb) : "memory","scc","v43","v42","s98","s99");
  }
  asm volatile("v_xor_b32 v43, 0x4040000, v44\n\tglobal_load_dwordx4 v[240:243], v43, %0 offset:0 nt\n\tglobal_load_dwordx4 v[244:247], v43, %0 offset:128 nt\n\tglobal_load_dwordx4 v[248:251], v43, %0 offset:512 nt\n\tglobal_load_dwordx4 v[252:255], v43, %0 offset:640 nt" :  : "s"(Kb) : "memory","scc","v43","v42","s98","s99");
  u32 t0_, t1_, t2_, t3_, t4_, t5_;
  asm volatile("s_mov_b32 vcc_lo, 0x30003\n\ts_mov_b32 vcc_hi, 0x30003\n\tv_max_f32_dpp %0, %10, %10 row_ror:8 row_mask:0xf bank_mask:0xf\n\tv_mfma_f32_16x16x32_bf16 v[28:31], %6, a[208:211], 0\n\tv_cndmask_b32 v36, 0, v52, vcc\n\tv_cndmask_b32 v37, 0, v53, vcc\n\tv_max_f32_dpp %0, %0, %0 row_ror:4 row_mask:0xf bank_mask:0xf\n\tv_cndmask_b32 v38, 0, v54, vcc\n\tv_cndmask_b32 v39, 0, v55, vcc\n\ts_lshl_b64 vcc, vcc, 2\n\tv_max_f32_dpp %0, %0, %0 row_ror:2 row_mask:0xf bank_mask:0xf\n\tv_cndmask_b32 v40, 0, v52, vcc\n\tv_cndmask_b32 v41, 0, v53, vcc\n\tv_max_f32_dpp %0, %0, %0 row_ror:1 row_mask:0xf bank_mask:0xf\n\tv_mfma_f32_16x16x32_bf16 v[32:35], %6, v[36:39], 0\n\tv_sub_f32 %0, %10, %0\n\tv_cndmask_b32 v42, 0, v54, vcc\n\tv_exp_f32 %0, %0\n\tv_cndmask_b32 v43, 0, v55, vcc\n\ts_lshl_b64 vcc, vcc, 2\n\tv_mfma_f32_16x16x32_bf16 v[28:31], %7, a[212:215], v[28:31]\n\tv_mul_f32 %0, %0, %11\n\tv_cndmask_b32 v36, 0, v52, vcc\n\tds_bpermute_b32 %2, %12, %0 offset:0\n\tds_bpermute_b32 %3, %12, %0 offset:4\n\tds_bpermute_b32 %4, %12, %0 offset:8\n\tds_bpermute_b32 %5, %12, %0 offset:12\n\tv_mfma_f32_16x16x32_bf16 v[32:35], %7, v[40:43], v[32:35]\n\tv_add_f32_dpp %1, %0, %0 row_ror:8 row_mask:0xf bank_mask:0xf\n\tv_cndmask_b32 v37, 0, v53, vcc\n\tv_cndmask_b32 v38, 0, v54, vcc\n\tv_add_f32_dpp %1, %1, %1 row_ror:4 row_mask:0xf bank_mask:0xf\n\tv_cndmask_b32 v39, 0, v55, vcc\n\ts_lshl_b64 vcc, vcc, 2\n\tv_mfma_f32_16x16x32_bf16 v[28:31], %8, a[216:219], v[28:31]\n\tv_add_f32_dpp %1, %1, %1 row_ror:2 row_mask:0xf bank_mask:0xf\n\tv_cndmask_b32 v40, 0, v52, vcc\n\tv_mfma_f32_16x16x32_bf16 v[32:35], %8, v[36:39], v[32:35]\n\tv_add_f32_dpp %1, %1, %1 row_ror:1 row_mask:0xf bank_mask:0xf\n\tv_cndmask_b32 v41, 0, v53, vcc\n\tv_cndmask_b32 v42, 0, v54, vcc\n\tv_cndmask_b32 v43, 0, v55, vcc\n\tv_mfma_f32_16x16x32_bf16 v[28:31], %9, a[220:223], v[28:31]\n\tv_rcp_f32 %1, %1\n\tv_mfma_f32_16x16x32_bf16 v[32:35], %9, v[40:43], v[32:35]\n\ts_nop 5\n\ts_waitcnt lgkmcnt(0)\n\tv_mul_f32 %0, %2, v28\n\tv_fmac_f32 %0, %3, v29\n\tv_fmac_f32 %0, %4, v30\n\tv_fmac_f32 %0, %5, v31\n\tv_mul_f32 %2, %2, v32\n\tv_fmac_f32 %2, %3, v33\n\tv_fmac_f32 %2, %4, v34\n\tv_fmac_f32 %2, %5, v35\n\ts_nop 1\n\tv_permlane32_swap_b32 %0, %2\n\ts_nop 0\n\tv_add_f32 %0, %0, %2\n\tv_mov_b32 %2, %0\n\ts_nop 1\n\tv_permlane16_swap_b32 %0, %2\n\ts_nop 0\n\tv_add_f32 %0, %0, %2\n\ts_nop 1\n\tv_add_f32_dpp %0, %0, %0 row_shl:2 row_mask:0x4 bank_mask:0xf\n\ts_nop 1\n\tv_add_f32_dpp %0, %0, %0 row_shl:4 row_mask:0x4 bank_mask:0xf\n\ts_mov_b32 exec_lo, 0xffff\n\ts_mov_b32 exec_hi, 3\n\tv_mul_f32 %0, %0, %1\n\tds_write_b32 %13, %0\n\ts_mov_b64 exec, -1\n\ts_waitcnt lgkmcnt(0)\n\ts_barrier" : "=&v"(t0_),"=&v"(t1_),"=&v"(t2_),"=&v"(t3_),"=&v"(t4_),"=&v"(t5_) : "v"(a0),"v"(a1),"v"(a2),"v"(a3),"v"(ml[0]),"v"(ml[1]),"v"(bpa),"v"(rda) : "memory","scc","vcc","v28","v29","v30","v31","v32","v33","v34","v35","v36","v37","v38","v39","v40","v41","v42","v43");
  tref = now32(); wo_finish(ln, w, yd); }
  asm volatile("ds_read_b128 a[240:243], v46 offset:16384\n\tds_read_b128 a[244:247], v46 offset:17408\n\tds_read_b128 a[248:251], v46 offset:18432\n\tds_read_b128 a[252:255], v46 offset:19456\n\ts_waitcnt lgkmcnt(0)" :  :  : "memory");
  {
  u32 ln = LANE(); u32 l40 = min(ln, 39u); u32 yo = w*640u + l40*16u; u64 yb = wsb + 720896ull + 2560ull, yb2 = yb + 7680ull, yb3 = yb + 15360ull;
  u32 xa = 9216u + (8u*w + l40/5u)*36u + (l40%5u)*8u; u32 x01 = *LDSP(u32, xa), x23 = *LDSP(u32, xa + 4);
  u32x4 y0,y1,y2,y3,y4,y5,y6,y7; u32 xb = l40 % 5u != 4u ? xa + 4u : ONES + 128u; f32x2 x0 = up2(x01), x2 = up2(x23);
  yb = pin(yb); yb2 = pin(yb2); yb3 = pin(yb3); asm volatile("" : "+v"(yo), "+v"(xa), "+v"(xb), "+v"(x0), "+v"(x2));
  pace(tref, 65);
  asm volatile("global_load_dwordx4 %0, %8, %9 offset:-2560 \n\tglobal_load_dwordx4 %1, %8, %9 offset:0 \n\tglobal_load_dwordx4 %2, %8, %9 offset:2560 \n\tglobal_load_dwordx4 %3, %8, %10 offset:-2560 \n\tglobal_load_dwordx4 %4, %8, %10 offset:0 \n\tglobal_load_dwordx4 %5, %8, %10 offset:2560 \n\tglobal_load_dwordx4 %6, %8, %11 offset:-2560 \n\tglobal_load_dwordx4 %7, %8, %11 offset:0 \n\ts_waitcnt vmcnt(0)" : "=&v"(y0),"=&v"(y1),"=&v"(y2),"=&v"(y3),"=&v"(y4),"=&v"(y5),"=&v"(y6),"=&v"(y7) : "v"(yo), "s"(yb), "s"(yb2), "s"(yb3) : "memory","scc","v43","v42","s98","s99");
  f32x4 ys = ((F4(y0)+F4(y1))+(F4(y2)+F4(y3))) + ((F4(y4)+F4(y5))+(F4(y6)+F4(y7))); if (__any(__builtin_isunordered(ys[0], ys[1]) || __builtin_isunordered(ys[2], ys[3]))) {
  _Pragma("unroll 1") for (int sp = 0; sp < SPIN; sp++) {
  asm volatile("global_load_dwordx4 %0, %8, %9 offset:-2560 sc1\n\tglobal_load_dwordx4 %1, %8, %9 offset:0 sc1\n\tglobal_load_dwordx4 %2, %8, %9 offset:2560 sc1\n\tglobal_load_dwordx4 %3, %8, %10 offset:-2560 sc1\n\tglobal_load_dwordx4 %4, %8, %10 offset:0 sc1\n\tglobal_load_dwordx4 %5, %8, %10 offset:2560 sc1\n\tglobal_load_dwordx4 %6, %8, %11 offset:-2560 sc1\n\tglobal_load_dwordx4 %7, %8, %11 offset:0 sc1\n\ts_waitcnt vmcnt(0)" : "=&v"(y0),"=&v"(y1),"=&v"(y2),"=&v"(y3),"=&v"(y4),"=&v"(y5),"=&v"(y6),"=&v"(y7) : "v"(yo), "s"(yb), "s"(yb2), "s"(yb3) : "memory","scc","v43","v42","s98","s99");
  u32 mx = umax3(umax3(umax4(y0), umax4(y1), umax4(y2)), umax3(umax4(y3), umax4(y4), umax4(y5)), max(umax4(y6), umax4(y7))); if (!__any(mx == 0xFFFFFFFFu)) break; __builtin_amdgcn_s_sleep(1); }
  ys = ((F4(y0)+F4(y1))+(F4(y2)+F4(y3))) + ((F4(y4)+F4(y5))+(F4(y6)+F4(y7))); }
  *LDSP(u32, xa) = pkv(x0 + f32x2{ys[0], ys[1]}); *LDSP(u32, xb) = pkv(x2 + f32x2{ys[2], ys[3]});
  BAR(); }
  asm volatile("v_xor_b32 v43, 0x4080000, v44\n\ts_add_u32 m0, %1, 16384\n\ts_nop 0\n\tglobal_load_lds_dwordx4 v43, %0 offset:0 nt\n\ts_add_u32 m0, %1, 17280\n\ts_nop 0\n\tglobal_load_lds_dwordx4 v43, %0 offset:128 nt\n\ts_add_u32 m0, %1, 17920\n\ts_nop 0\n\tglobal_load_lds_dwordx4 v43, %0 offset:512 nt\n\ts_add_u32 m0, %1, 18816\n\ts_nop 0\n\tglobal_load_lds_dwordx4 v43, %0 offset:640 nt" :  : "s"(Kb), "s"(lb) : "memory","scc","v43","v42","s98","s99");
  const u32 d16 = 944u + min((u32)max((int)(now32() - t0) - 2700, 0) * 32u >> 5, 72u);
  { u32 ln = LANE(); u32x4 xp; u32 xt;
  u32 ha = (ln&1u) ? ONES : 11520u + (s>>2)*288u + (ln>>4)*72u; u64 qd = pin(wsb + 802816ull + (u64)(kvh*4096 + (s&3)*1024 + w*256 + (s>>2)*4));
  asm volatile("" : "+v"(ha));
  mixer(9216u, ln, xp, xt);
  *LDSP(u32x4, MRG + ln*16u) = xp; *LDSP(u16, MRG + 1024u + ln*2u) = (u16)xt;
  u32x4 f0 = ld8x2(ha), f1 = ld8x2(ha+16), f2 = ld8x2(ha+32), f3 = ld8x2(ha+48); u32x2 f4 = *LDSP(u32x2, ha+64); f32x4 dq, de; Cen c = stats1(xp, xt);
  asm volatile("s_nop 1\n\tv_mfma_f32_16x16x32_bf16 %0, %4, a[224:227], 0\n\tv_mfma_f32_16x16x32_bf16 %0, %5, a[228:231], %0\n\tv_mfma_f32_16x16x32_bf16 %0, %6, a[232:235], %0\n\tv_mfma_f32_16x16x32_bf16 %0, %7, a[236:239], %0\n\tv_mfma_f32_16x16x16_bf16 %1, %8, v[58:59], 0" : "=&v"(dq), "=&v"(de), "+v"(c.s1), "+v"(c.s2) : "v"(f0),"v"(f1),"v"(f2),"v"(f3),"v"(f4) : "memory");
  stats2(c); float rs = __builtin_amdgcn_rsqf(c.var + 1e-5f), mn = c.pv + c.md; asm volatile("" : "+v"(rs), "+v"(mn), "+v"(dq), "+v"(de));
  if (ln < 16u) st4(ln*16u, nz(__float_as_uint(fmaf(-mn, dq[1] + de[1], dq[0] + de[0]) * rs)), qd);
  xsave = xp; xsave_t = xt; }
  {
  u32 ln = LANE(); u32 qo = ln*16u; u64 qb_ = wsb + 802816ull + (u64)(kvh*4096 + w*1024); u32x4 a0;
  delay(25);
  _Pragma("unroll 1") for (int sp = 0; sp < SPIN; sp++) {
  if (sp) { asm volatile("global_load_dwordx4 %0, %1, %2 sc1\n\ts_waitcnt vmcnt(0)" : "=&v"(a0) : "v"(qo), "s"(qb_) : "memory"); } else { asm volatile("global_load_dwordx4 %0, %1, %2 \n\ts_waitcnt vmcnt(0)" : "=&v"(a0) : "v"(qo), "s"(qb_) : "memory","scc","v43","v42","s98","s99"); }
  if (!__any(umax4(a0) == 0xFFFFFFFFu)) break; __builtin_amdgcn_s_sleep(1); }
  f32x4 v = F4(a0); *LDSP(u16, 8704u + qpos(w, ln)) = (u16)pk2((v[0]+v[1])+(v[2]+v[3]), 0.f);
  BAR();
  if (w == 3) { *LDSP(u32x4, 9216u + ln*16u) = xsave; *LDSP(u16, 10240u + ln*2u) = (u16)xsave_t; } }
  tb = now32() - 15u;
  asm volatile("v_mov_b32 v40, 7264\n\ts_mov_b32 vcc_lo, 0x11111111\n\ts_mov_b32 vcc_hi, 0x11111111\n\tv_cndmask_b32 v4, v40, v47, vcc\n\ts_lshl_b64 vcc, vcc, 1\n\tv_cndmask_b32 v5, v40, v47, vcc\n\ts_lshl_b64 vcc, vcc, 1\n\tv_cndmask_b32 v6, v40, v47, vcc\n\ts_lshl_b64 vcc, vcc, 1\n\tv_cndmask_b32 v7, v40, v47, vcc\n\tds_read_b128 v[8:11], v4 offset:8704\n\tds_read_b128 v[12:15], v4 offset:8720\n\tds_read_b128 v[16:19], v5 offset:8704\n\tds_read_b128 v[20:23], v5 offset:8720\n\tds_read_b128 v[24:27], v6 offset:8704\n\tds_read_b128 v[28:31], v6 offset:8720\n\tds_read_b128 v[32:35], v7 offset:8704\n\tds_read_b128 v[36:39], v7 offset:8720\n\ts_waitcnt lgkmcnt(6)\n\tv_mfma_f32_16x16x32_bf16 v[4:7], v[64:67], v[8:11], 0\n\tv_mfma_f32_16x16x32_bf16 v[40:43], v[72:75], v[8:11], 0\n\tv_mfma_f32_16x16x32_bf16 v[4:7], v[68:71], v[12:15], v[4:7]\n\tv_mfma_f32_16x16x32_bf16 v[40:43], v[76:79], v[12:15], v[40:43]\n\ts_waitcnt lgkmcnt(4)\n\tv_mfma_f32_16x16x32_bf16 v[4:7], v[80:83], v[16:19], v[4:7]\n\tv_mfma_f32_16x16x32_bf16 v[40:43], v[88:91], v[16:19], v[40:43]\n\tv_mfma_f32_16x16x32_bf16 v[4:7], v[84:87], v[20:23], v[4:7]\n\tv_mfma_f32_16x16x32_bf16 v[40:43], v[92:95], v[20:23], v[40:43]\n\tds_read_b128 v[80:83], v46 offset:20480\n\tds_read_b128 v[84:87], v46 offset:21504\n\tds_read_b128 v[88:91], v46 offset:22528\n\tds_read_b128 v[92:95], v46 offset:23552\n\ts_waitcnt lgkmcnt(6)\n\tv_mfma_f32_16x16x32_bf16 v[4:7], v[96:99], v[24:27], v[4:7]\n\tv_mfma_f32_16x16x32_bf16 v[40:43], v[104:107], v[24:27], v[40:43]\n\tv_mfma_f32_16x16x32_bf16 v[4:7], v[100:103], v[28:31], v[4:7]\n\tv_mfma_f32_16x16x32_bf16 v[40:43], v[108:111], v[28:31], v[40:43]\n\tds_read_b128 v[96:99], v46 offset:24576\n\tds_read_b128 v[100:103], v46 offset:25600\n\tds_read_b128 v[104:107], v46 offset:26624\n\tds_read_b128 v[108:111], v46 offset:27648\n\ts_waitcnt lgkmcnt(8)\n\tv_mfma_f32_16x16x32_bf16 v[4:7], v[112:115], v[32:35], v[4:7]\n\tv_mfma_f32_16x16x32_bf16 v[40:43], v[120:123], v[32:35], v[40:43]\n\tv_mfma_f32_16x16x32_bf16 v[4:7], v[116:119], v[36:39], v[4:7]\n\tv_mfma_f32_16x16x32_bf16 v[40:43], v[124:127], v[36:39], v[40:43]\n\tds_read_b128 v[112:115], v46 offset:28672\n\tds_read_b128 v[116:119], v46 offset:29696\n\tds_read_b128 v[120:123], v46 offset:30720\n\tds_read_b128 v[124:127], v46 offset:31744\n\ts_waitcnt lgkmcnt(8)\n\tv_xor_b32 v57, 0x40c0000, v44\n\ts_add_u32 m0, %2, 20480\n\ts_nop 0\n\tglobal_load_lds_dwordx4 v57, %1 offset:0 nt\n\ts_add_u32 m0, %2, 21376\n\ts_nop 0\n\tglobal_load_lds_dwordx4 v57, %1 offset:128 nt\n\ts_add_u32 m0, %2, 22016\n\ts_nop 0\n\tglobal_load_lds_dwordx4 v57, %1 offset:512 nt\n\ts_add_u32 m0, %2, 22912\n\ts_nop 0\n\tglobal_load_lds_dwordx4 v57, %1 offset:640 nt\n\tv_mfma_f32_16x16x32_bf16 v[72:75], v[192:195], v[8:11], 0\n\tv_mfma_f32_16x16x32_bf16 v[76:79], v[200:203], v[8:11], 0\n\tv_mfma_f32_16x16x32_bf16 v[72:75], v[196:199], v[12:15], v[72:75]\n\tv_mfma_f32_16x16x32_bf16 v[76:79], v[204:207], v[12:15], v[76:79]\n\tds_read_b128 v[192:195], v46 offset:32768\n\tds_read_b128 v[196:199], v46 offset:33792\n\tds_read_b128 v[200:203], v46 offset:34816\n\tds_read_b128 v[204:207], v46 offset:35840\n\tv_mfma_f32_16x16x32_bf16 v[72:75], v[208:211], v[16:19], v[72:75]\n\tv_add_f32_dpp v64, v5, v4 row_ror:8 row_mask:0xf bank_mask:0x3\n\tv_mfma_f32_16x16x32_bf16 v[76:79], v[216:219], v[16:19], v[76:79]\n\tv_add_f32_dpp v65, v7, v6 row_ror:8 row_mask:0xf bank_mask:0x3\n\tv_mfma_f32_16x16x32_bf16 v[72:75], v[212:215], v[20:23], v[72:75]\n\tv_add_f32_dpp v66, v41, v40 row_ror:8 row_mask:0xf bank_mask:0x3\n\tv_mfma_f32_16x16x32_bf16 v[76:79], v[220:223], v[20:23], v[76:79]\n\tv_add_f32_dpp v67, v43, v42 row_ror:8 row_mask:0xf bank_mask:0x3\n\tds_read_b128 v[208:211], v46 offset:0\n\tds_read_b128 v[212:215], v46 offset:1024\n\tds_read_b128 v[216:219], v46 offset:2048\n\tds_read_b128 v[220:223], v46 offset:3072\n\tv_mfma_f32_16x16x32_bf16 v[72:75], v[224:227], v[24:27], v[72:75]\n\tv_add_f32_dpp v64, v4, v5 row_ror:8 row_mask:0xf bank_mask:0xc\n\tv_mfma_f32_16x16x32_bf16 v[76:79], v[232:235], v[24:27], v[76:79]\n\tv_add_f32_dpp v65, v6, v7 row_ror:8 row_mask:0xf bank_mask:0xc\n\tv_mfma_f32_16x16x32_bf16 v[72:75], v[228:231], v[28:31], v[72:75]\n\tv_add_f32_dpp v66, v40, v41 row_ror:8 row_mask:0xf bank_mask:0xc\n\tv_mfma_f32_16x16x32_bf16 v[76:79], v[236:239], v[28:31], v[76:79]\n\tv_add_f32_dpp v67, v42, v43 row_ror:8 row_mask:0xf bank_mask:0xc\n\tds_read_b128 v[224:227], v46 offset:4096\n\tds_read_b128 v[228:231], v46 offset:5120\n\tds_read_b128 v[232:235], v46 offset:6144\n\tds_read_b128 v[236:239], v46 offset:7168\n\tv_mfma_f32_16x16x32_bf16 v[72:75], a[240:243], v[32:35], v[72:75]\n\tv_max3_f32 v56, v64, v65, v66\n\tv_mfma_f32_16x16x32_bf16 v[76:79], a[248:251], v[32:35], v[76:79]\n\tv_max_f32 v56, v56, v67\n\tv_mfma_f32_16x16x32_bf16 v[72:75], a[244:247], v[36:39], v[72:75]\n\tv_mfma_f32_16x16x32_bf16 v[76:79], a[252:255], v[36:39], v[76:79]\n\tds_read_b128 a[240:243], v46 offset:8192\n\tds_read_b128 a[244:247], v46 offset:9216\n\tds_read_b128 a[248:251], v46 offset:10240\n\tds_read_b128 a[252:255], v46 offset:11264\n\tv_mfma_f32_16x16x32_bf16 v[4:7], a[128:131], v[8:11], 0\n\tv_mfma_f32_16x16x32_bf16 v[40:43], a[136:139], v[8:11], 0\n\tv_mfma_f32_16x16x32_bf16 v[4:7], a[132:135], v[12:15], v[4:7]\n\tv_mfma_f32_16x16x32_bf16 v[40:43], a[140:143], v[12:15], v[40:43]\n\ts_waitcnt lgkmcnt(15)\n\tv_mfma_f32_16x16x32_bf16 v[4:7], v[80:83], v[16:19], v[4:7]\n\tv_add_f32_dpp v68, v73, v72 row_ror:8 row_mask:0xf bank_mask:0x3\n\tv_mfma_f32_16x16x32_bf16 v[40:43], v[88:91], v[16:19], v[40:43]\n\tv_add_f32_dpp v69, v75, v74 row_ror:8 row_mask:0xf bank_mask:0x3\n\tv_mfma_f32_16x16x32_bf16 v[4:7], v[84:87], v[20:23], v[4:7]\n\tv_add_f32_dpp v70, v77, v76 row_ror:8 row_mask:0xf bank_mask:0x3\n\tv_mfma_f32_16x16x32_bf16 v[40:43], v[92:95], v[20:23], v[40:43]\n\tv_add_f32_dpp v71, v79, v78 row_ror:8 row_mask:0xf bank_mask:0x3\n\ts_waitcnt lgkmcnt(15)\n\tv_mfma_f32_16x16x32_bf16 v[4:7], v[96:99], v[24:27], v[4:7]\n\tv_add_f32_dpp v68, v72, v73 row_ror:8 row_mask:0xf bank_mask:0xc\n\tv_mfma_f32_16x16x32_bf16 v[40:43], v[104:107], v[24:27], v[40:43]\n\tv_add_f32_dpp v69, v74, v75 row_ror:8 row_mask:0xf bank_mask:0xc\n\tv_mfma_f32_16x16x32_bf16 v[4:7], v[100:103], v[28:31], v[4:7]\n\tv_add_f32_dpp v70, v76, v77 row_ror:8 row_mask:0xf bank_mask:0xc\n\tv_mfma_f32_16x16x32_bf16 v[40:43], v[108:111], v[28:31], v[40:43]\n\tv_add_f32_dpp v71, v78, v79 row_ror:8 row_mask:0xf bank_mask:0xc\n\tv_xor_b32 v57, 0x4000000, v45\n\tglobal_load_dwordx4 a[128:131], v57, %3 offset:0 nt\n\tglobal_load_dwordx4 a[132:135], v57, %3 offset:256 nt\n\tglobal_load_dwordx4 a[136:139], v57, %3 offset:512 nt\n\tglobal_load_dwordx4 a[140:143], v57, %3 offset:768 nt\n\ts_waitcnt lgkmcnt(15)\n\tv_mfma_f32_16x16x32_bf16 v[4:7], v[112:115], v[32:35], v[4:7]\n\tv_max3_f32 v56, v68, v69, v56\n\tv_mfma_f32_16x16x32_bf16 v[40:43], v[120:123], v[32:35], v[40:43]\n\tv_max3_f32 v56, v70, v71, v56\n\tv_mfma_f32_16x16x32_bf16 v[4:7], v[116:119], v[36:39], v[4:7]\n\tv_mfma_f32_16x16x32_bf16 v[40:43], v[124:127], v[36:39], v[40:43]\n\ts_waitcnt lgkmcnt(12)\n\tv_mfma_f32_16x16x32_bf16 v[72:75], v[192:195], v[8:11], 0\n\tv_mfma_f32_16x16x32_bf16 v[76:79], v[200:203], v[8:11], 0\n\tv_mfma_f32_16x16x32_bf16 v[72:75], v[196:199], v[12:15], v[72:75]\n\tv_mfma_f32_16x16x32_bf16 v[76:79], v[204:207], v[12:15], v[76:79]\n\ts_waitcnt lgkmcnt(8)\n\tv_mfma_f32_16x16x32_bf16 v[72:75], v[208:211], v[16:19], v[72:75]\n\tv_add_f32_dpp v48, v5, v4 row_ror:8 row_mask:0xf bank_mask:0x3\n\tv_mfma_f32_16x16x32_bf16 v[76:79], v[216:219], v[16:19], v[76:79]\n\tv_add_f32_dpp v49, v7, v6 row_ror:8 row_mask:0xf bank_mask:0x3\n\tv_mfma_f32_16x16x32_bf16 v[72:75], v[212:215], v[20:23], v[72:75]\n\tv_add_f32_dpp v50, v41, v40 row_ror:8 row_mask:0xf bank_mask:0x3\n\tv_mfma_f32_16x16x32_bf16 v[76:79], v[220:223], v[20:23], v[76:79]\n\tv_add_f32_dpp v51, v43, v42 row_ror:8 row_mask:0xf bank_mask:0x3\n\ts_waitcnt lgkmcnt(4)\n\tv_mfma_f32_16x16x32_bf16 v[72:75], v[224:227], v[24:27], v[72:75]\n\tv_add_f32_dpp v48, v4, v5 row_ror:8 row_mask:0xf bank_mask:0xc\n\tv_mfma_f32_16x16x32_bf16 v[76:79], v[232:235], v[24:27], v[76:79]\n\tv_add_f32_dpp v49, v6, v7 row_ror:8 row_mask:0xf bank_mask:0xc\n\tv_mfma_f32_16x16x32_bf16 v[72:75], v[228:231], v[28:31], v[72:75]\n\tv_add_f32_dpp v50, v40, v41 row_ror:8 row_mask:0xf bank_mask:0xc\n\tv_mfma_f32_16x16x32_bf16 v[76:79], v[236:239], v[28:31], v[76:79]\n\tv_add_f32_dpp v51, v42, v43 row_ror:8 row_mask:0xf bank_mask:0xc\n\ts_waitcnt lgkmcnt(0)\n\tv_mfma_f32_16x16x32_bf16 v[72:75], a[240:243], v[32:35], v[72:75]\n\tv_max3_f32 v56, v48, v49, v56\n\tv_mfma_f32_16x16x32_bf16 v[76:79], a[248:251], v[32:35], v[76:79]\n\tv_max3_f32 v56, v50, v51, v56\n\tv_mfma_f32_16x16x32_bf16 v[72:75], a[244:247], v[36:39], v[72:75]\n\tv_mfma_f32_16x16x32_bf16 v[76:79], a[252:255], v[36:39], v[76:79]\n\ts_nop 7\n\ts_nop 1\n\tv_add_f32_dpp v8, v73, v72 row_ror:8 row_mask:0xf bank_mask:0x3\n\tv_add_f32_dpp v9, v75, v74 row_ror:8 row_mask:0xf bank_mask:0x3\n\tv_add_f32_dpp v10, v77, v76 row_ror:8 row_mask:0xf bank_mask:0x3\n\tv_add_f32_dpp v11, v79, v78 row_ror:8 row_mask:0xf bank_mask:0x3\n\tv_add_f32_dpp v8, v72, v73 row_ror:8 row_mask:0xf bank_mask:0xc\n\tv_add_f32_dpp v9, v74, v75 row_ror:8 row_mask:0xf bank_mask:0xc\n\tv_add_f32_dpp v10, v76, v77 row_ror:8 row_mask:0xf bank_mask:0xc\n\tv_add_f32_dpp v11, v78, v79 row_ror:8 row_mask:0xf bank_mask:0xc\n\tv_max3_f32 v56, v8, v9, v56\n\tv_max3_f32 v56, v10, v11, v56\n\ts_nop 1\n\tv_max_f32_dpp v56, v56, v56 quad_perm:[1,0,3,2] row_mask:0xf bank_mask:0xf\n\ts_nop 1\n\tv_max_f32_dpp v56, v56, v56 quad_perm:[2,3,0,1] row_mask:0xf bank_mask:0xf\n\tv_mov_b32 v13, v56\n\ts_nop 1\n\tv_permlane16_swap_b32 v56, v13\n\ts_nop 0\n\tv_max_f32 v56, v56, v13\n\tv_mov_b32 v13, v56\n\ts_nop 1\n\tv_permlane32_swap_b32 v56, v13\n\ts_nop 0\n\tv_max_f32 v56, v56, v13\n\tv_mul_f32 v15, %0, v56\n\tv_fma_f32 v64, v64, %0, -v15\n\tv_fma_f32 v65, v65, %0, -v15\n\tv_fma_f32 v66, v66, %0, -v15\n\tv_fma_f32 v67, v67, %0, -v15\n\tv_fma_f32 v68, v68, %0, -v15\n\tv_fma_f32 v69, v69, %0, -v15\n\tv_fma_f32 v70, v70, %0, -v15\n\tv_fma_f32 v71, v71, %0, -v15\n\tv_fma_f32 v48, v48, %0, -v15\n\tv_fma_f32 v49, v49, %0, -v15\n\tv_fma_f32 v50, v50, %0, -v15\n\tv_fma_f32 v51, v51, %0, -v15\n\tv_fma_f32 v8, v8, %0, -v15\n\tv_fma_f32 v9, v9, %0, -v15\n\tv_fma_f32 v10, v10, %0, -v15\n\tv_fma_f32 v11, v11, %0, -v15\n\tv_exp_f32 v64, v64\n\tv_exp_f32 v65, v65\n\tv_exp_f32 v66, v66\n\tv_exp_f32 v67, v67\n\tv_exp_f32 v68, v68\n\tv_exp_f32 v69, v69\n\tv_exp_f32 v70, v70\n\tv_exp_f32 v71, v71\n\tv_exp_f32 v48, v48\n\tv_exp_f32 v49, v49\n\tv_exp_f32 v50, v50\n\tv_exp_f32 v51, v51\n\tv_exp_f32 v8, v8\n\tv_exp_f32 v9, v9\n\tv_exp_f32 v10, v10\n\tv_exp_f32 v11, v11\n\tv_cvt_pk_bf16_f32 v24, v64, v64\n\tv_cvt_pk_bf16_f32 v25, v65, v65\n\tv_cvt_pk_bf16_f32 v26, v66, v66\n\tv_cvt_pk_bf16_f32 v27, v67, v67\n\tv_cvt_pk_bf16_f32 v28, v68, v68\n\tv_cvt_pk_bf16_f32 v29, v69, v69\n\tv_cvt_pk_bf16_f32 v30, v70, v70\n\tv_cvt_pk_bf16_f32 v31, v71, v71\n\tv_cvt_pk_bf16_f32 v32, v48, v48\n\tv_cvt_pk_bf16_f32 v33, v49, v49\n\tv_cvt_pk_bf16_f32 v34, v50, v50\n\tv_cvt_pk_bf16_f32 v35, v51, v51\n\tv_cvt_pk_bf16_f32 v36, v8, v8\n\tv_cvt_pk_bf16_f32 v37, v9, v9\n\tv_cvt_pk_bf16_f32 v38, v10, v10\n\tv_cvt_pk_bf16_f32 v39, v11, v11\n\tv_pk_add_f32 v[64:65], v[64:65], v[66:67]\n\tv_pk_add_f32 v[68:69], v[68:69], v[70:71]\n\tv_pk_add_f32 v[48:49], v[48:49], v[50:51]\n\tv_pk_add_f32 v[8:9], v[8:9], v[10:11]\n\tv_pk_add_f32 v[64:65], v[64:65], v[68:69]\n\tv_pk_add_f32 v[8:9], v[8:9], v[48:49]\n\tv_pk_add_f32 v[8:9], v[8:9], v[64:65]\n\tv_add_f32 v57, v8, v9\n\tv_mbcnt_lo_u32_b32 v40, -1, 0\n\tv_mbcnt_hi_u32_b32 v40, -1, v40\n\tv_add_f32_dpp v41, v57, v57 quad_perm:[1,0,3,2] row_mask:0xf bank_mask:0xf\n\tv_and_b32 v42, 4, v40\n\tv_lshrrev_b32 v43, 4, v40\n\tv_add_f32_dpp v41, v41, v41 quad_perm:[2,3,0,1] row_mask:0xf bank_mask:0xf\n\tv_add_u32 v42, v42, v43\n\tv_mov_b32 v43, v41\n\tv_lshl_add_u32 v13, v42, 7, %4\n\tv_and_b32 v42, 8, v40\n\tv_permlane16_swap_b32 v41, v43\n\tv_lshl_add_u32 v13, v42, 1, v13\n\tv_add_f32 v41, v41, v43\n\tv_and_b32 v42, 3, v40\n\tv_mov_b32 v43, v41\n\tv_lshl_add_u32 v13, v42, 2, v13\n\tv_lshl_add_u32 v12, v40, 3, %5\n\tv_permlane32_swap_b32 v41, v43\n\tv_mul_f32 v40, %0, v56\n\tv_add_f32 v41, v41, v43\n\ts_mov_b64 exec, 0x11\n\tds_write_b64 v12, v[40:41]\n\ts_mov_b64 exec, -1\n\tv_and_b32_dpp v16, v24, v60 quad_perm:[0,0,0,0] row_mask:0xf bank_mask:0xf\n\tv_and_b32_dpp v17, v24, v61 quad_perm:[0,0,0,0] row_mask:0xf bank_mask:0xf\n\tv_and_b32_dpp v18, v24, v62 quad_perm:[0,0,0,0] row_mask:0xf bank_mask:0xf\n\tv_and_b32_dpp v19, v24, v63 quad_perm:[0,0,0,0] row_mask:0xf bank_mask:0xf\n\tv_and_b32_dpp v20, v25, v60 quad_perm:[0,0,0,0] row_mask:0xf bank_mask:0xf\n\tv_and_b32_dpp v21, v25, v61 quad_perm:[0,0,0,0] row_mask:0xf bank_mask:0xf\n\tv_and_b32_dpp v22, v25, v62 quad_perm:[0,0,0,0] row_mask:0xf bank_mask:0xf\n\tv_and_b32_dpp v23, v25, v63 quad_perm:[0,0,0,0] row_mask:0xf bank_mask:0xf\n\tv_mfma_f32_16x16x32_bf16 v[48:51], v[128:131], v[16:19], 0\n\tv_and_b32_dpp v16, v26, v60 quad_perm:[0,0,0,0] row_mask:0xf bank_mask:0xf\n\tv_and_b32_dpp v17, v26, v61 quad_perm:[0,0,0,0] row_mask:0xf bank_mask:0xf\n\tv_and_b32_dpp v18, v26, v62 quad_perm:[0,0,0,0] row_mask:0xf bank_mask:0xf\n\tv_and_b32_dpp v19, v26, v63 quad_perm:[0,0,0,0] row_mask:0xf bank_mask:0xf\n\tv_mfma_f32_16x16x32_bf16 v[48:51], v[132:135], v[20:23], v[48:51]\n\tv_and_b32_dpp v20, v27, v60 quad_perm:[0,0,0,0] row_mask:0xf bank_mask:0xf\n\tv_and_b32_dpp v21, v27, v61 quad_perm:[0,0,0,0] row_mask:0xf bank_mask:0xf\n\tv_and_b32_dpp v22, v27, v62 quad_perm:[0,0,0,0] row_mask:0xf bank_mask:0xf\n\tv_and_b32_dpp v23, v27, v63 quad_perm:[0,0,0,0] row_mask:0xf bank_mask:0xf\n\tv_mfma_f32_16x16x32_bf16 v[48:51], v[136:139], v[16:19], v[48:51]\n\tv_and_b32_dpp v16, v24, v60 quad_perm:[1,1,1,1] row_mask:0xf bank_mask:0xf\n\tv_and_b32_dpp v17, v24, v61 quad_perm:[1,1,1,1] row_mask:0xf bank_mask:0xf\n\tv_and_b32_dpp v18, v24, v62 quad_perm:[1,1,1,1] row_mask:0xf bank_mask:0xf\n\tv_and_b32_dpp v19, v24, v63 quad_perm:[1,1,1,1] row_mask:0xf bank_mask:0xf\n\tv_mfma_f32_16x16x32_bf16 v[48:51], v[140:143], v[20:23], v[48:51]\n\tv_and_b32_dpp v20, v25, v60 quad_perm:[1,1,1,1] row_mask:0xf bank_mask:0xf\n\tv_and_b32_dpp v21, v25, v61 quad_perm:[1,1,1,1] row_mask:0xf bank_mask:0xf\n\tv_and_b32_dpp v22, v25, v62 quad_perm:[1,1,1,1] row_mask:0xf bank_mask:0xf\n\tv_and_b32_dpp v23, v25, v63 quad_perm:[1,1,1,1] row_mask:0xf bank_mask:0xf\n\tv_mfma_f32_16x16x32_bf16 v[48:51], v[144:147], v[16:19], v[48:51]\n\tv_and_b32_dpp v16, v26, v60 quad_perm:[1,1,1,1] row_mask:0xf bank_mask:0xf\n\tv_and_b32_dpp v17, v26, v61 quad_perm:[1,1,1,1] row_mask:0xf bank_mask:0xf\n\tv_and_b32_dpp v18, v26, v62 quad_perm:[1,1,1,1] row_mask:0xf bank_mask:0xf\n\tv_and_b32_dpp v19, v26, v63 quad_perm:[1,1,1,1] row_mask:0xf bank_mask:0xf\n\tv_mfma_f32_16x16x32_bf16 v[48:51], v[148:151], v[20:23], v[48:51]\n\tv_and_b32_dpp v20, v27, v60 quad_perm:[1,1,1,1] row_mask:0xf bank_mask:0xf\n\tv_and_b32_dpp v21, v27, v61 quad_perm:[1,1,1,1] row_mask:0xf bank_mask:0xf\n\tv_and_b32_dpp v22, v27, v62 quad_perm:[1,1,1,1] row_mask:0xf bank_mask:0xf\n\tv_and_b32_dpp v23, v27, v63 quad_perm:[1,1,1,1] row_mask:0xf bank_mask:0xf\n\tv_mfma_f32_16x16x32_bf16 v[48:51], v[152:155], v[16:19], v[48:51]\n\tv_and_b32_dpp v16, v24, v60 quad_perm:[2,2,2,2] row_mask:0xf bank_mask:0xf\n\tv_and_b32_dpp v17, v24, v61 quad_perm:[2,2,2,2] row_mask:0xf bank_mask:0xf\n\tv_and_b32_dpp v18, v24, v62 quad_perm:[2,2,2,2] row_mask:0xf bank_mask:0xf\n\tv_and_b32_dpp v19, v24, v63 quad_perm:[2,2,2,2] row_mask:0xf bank_mask:0xf\n\tv_mfma_f32_16x16x32_bf16 v[48:51], v[156:159], v[20:23], v[48:51]\n\tv_xor_b32 v14, 0x4040000, v45\n\tglobal_load_dwordx4 v[80:83], v14, %3 offset:0 nt\n\tglobal_load_dwordx4 v[84:87], v14, %3 offset:256 nt\n\tglobal_load_dwordx4 v[88:91], v14, %3 offset:512 nt\n\tglobal_load_dwordx4 v[92:95], v14, %3 offset:768 nt\n\tv_and_b32_dpp v20, v25, v60 quad_perm:[2,2,2,2] row_mask:0xf bank_mask:0xf\n\tv_and_b32_dpp v21, v25, v61 quad_perm:[2,2,2,2] row_mask:0xf bank_mask:0xf\n\tv_and_b32_dpp v22, v25, v62 quad_perm:[2,2,2,2] row_mask:0xf bank_mask:0xf\n\tv_and_b32_dpp v23, v25, v63 quad_perm:[2,2,2,2] row_mask:0xf bank_mask:0xf\n\tv_mfma_f32_16x16x32_bf16 v[48:51], v[160:163], v[16:19], v[48:51]\n\tv_and_b32_dpp v16, v26, v60 quad_perm:[2,2,2,2] row_mask:0xf bank_mask:0xf\n\tv_and_b32_dpp v17, v26, v61 quad_perm:[2,2,2,2] row_mask:0xf bank_mask:0xf\n\tv_and_b32_dpp v18, v26, v62 quad_perm:[2,2,2,2] row_mask:0xf bank_mask:0xf\n\tv_and_b32_dpp v19, v26, v63 quad_perm:[2,2,2,2] row_mask:0xf bank_mask:0xf\n\tv_mfma_f32_16x16x32_bf16 v[48:51], v[164:167], v[20:23], v[48:51]\n\tv_and_b32_dpp v20, v27, v60 quad_perm:[2,2,2,2] row_mask:0xf bank_mask:0xf\n\tv_and_b32_dpp v21, v27, v61 quad_perm:[2,2,2,2] row_mask:0xf bank_mask:0xf\n\tv_and_b32_dpp v22, v27, v62 quad_perm:[2,2,2,2] row_mask:0xf bank_mask:0xf\n\tv_and_b32_dpp v23, v27, v63 quad_perm:[2,2,2,2] row_mask:0xf bank_mask:0xf\n\tv_mfma_f32_16x16x32_bf16 v[48:51], v[168:171], v[16:19], v[48:51]\n\tv_and_b32_dpp v16, v24, v60 quad_perm:[3,3,3,3] row_mask:0xf bank_mask:0xf\n\tv_and_b32_dpp v17, v24, v61 quad_perm:[3,3,3,3] row_mask:0xf bank_mask:0xf\n\tv_and_b32_dpp v18, v24, v62 quad_perm:[3,3,3,3] row_mask:0xf bank_mask:0xf\n\tv_and_b32_dpp v19, v24, v63 quad_perm:[3,3,3,3] row_mask:0xf bank_mask:0xf\n\tv_mfma_f32_16x16x32_bf16 v[48:51], v[172:175], v[20:23], v[48:51]\n\tv_and_b32_dpp v20, v25, v60 quad_perm:[3,3,3,3] row_mask:0xf bank_mask:0xf\n\tv_and_b32_dpp v21, v25, v61 quad_perm:[3,3,3,3] row_mask:0xf bank_mask:0xf\n\tv_and_b32_dpp v22, v25, v62 quad_perm:[3,3,3,3] row_mask:0xf bank_mask:0xf\n\tv_and_b32_dpp v23, v25, v63 quad_perm:[3,3,3,3] row_mask:0xf bank_mask:0xf\n\tv_mfma_f32_16x16x32_bf16 v[48:51], v[176:179], v[16:19], v[48:51]\n\tv_and_b32_dpp v16, v26, v60 quad_perm:[3,3,3,3] row_mask:0xf bank_mask:0xf\n\tv_and_b32_dpp v17, v26, v61 quad_perm:[3,3,3,3] row_mask:0xf bank_mask:0xf\n\tv_and_b32_dpp v18, v26, v62 quad_perm:[3,3,3,3] row_mask:0xf bank_mask:0xf\n\tv_and_b32_dpp v19, v26, v63 quad_perm:[3,3,3,3] row_mask:0xf bank_mask:0xf\n\tv_mfma_f32_16x16x32_bf16 v[48:51], v[180:183], v[20:23], v[48:51]\n\tv_and_b32_dpp v20, v27, v60 quad_perm:[3,3,3,3] row_mask:0xf bank_mask:0xf\n\tv_and_b32_dpp v21, v27, v61 quad_perm:[3,3,3,3] row_mask:0xf bank_mask:0xf\n\tv_and_b32_dpp v22, v27, v62 quad_perm:[3,3,3,3] row_mask:0xf bank_mask:0xf\n\tv_and_b32_dpp v23, v27, v63 quad_perm:[3,3,3,3] row_mask:0xf bank_mask:0xf\n\tv_mfma_f32_16x16x32_bf16 v[48:51], v[184:187], v[16:19], v[48:51]\n\tv_and_b32_dpp v16, v28, v60 quad_perm:[0,0,0,0] row_mask:0xf bank_mask:0xf\n\tv_and_b32_dpp v17, v28, v61 quad_perm:[0,0,0,0] row_mask:0xf bank_mask:0xf\n\tv_and_b32_dpp v18, v28, v62 quad_perm:[0,0,0,0] row_mask:0xf bank_mask:0xf\n\tv_and_b32_dpp v19, v28, v63 quad_perm:[0,0,0,0] row_mask:0xf bank_mask:0xf\n\tv_mfma_f32_16x16x32_bf16 v[48:51], v[188:191], v[20:23], v[48:51]\n\tv_and_b32_dpp v20, v29, v60 quad_perm:[0,0,0,0] row_mask:0xf bank_mask:0xf\n\tv_and_b32_dpp v21, v29, v61 quad_perm:[0,0,0,0] row_mask:0xf bank_mask:0xf\n\tv_and_b32_dpp v22, v29, v62 quad_perm:[0,0,0,0] row_mask:0xf bank_mask:0xf\n\tv_and_b32_dpp v23, v29, v63 quad_perm:[0,0,0,0] row_mask:0xf bank_mask:0xf\n\tv_mfma_f32_16x16x32_bf16 v[48:51], a[0:3], v[16:19], v[48:51]\n\tv_and_b32_dpp v16, v30, v60 quad_perm:[0,0,0,0] row_mask:0xf bank_mask:0xf\n\tv_and_b32_dpp v17, v30, v61 quad_perm:[0,0,0,0] row_mask:0xf bank_mask:0xf\n\tv_and_b32_dpp v18, v30, v62 quad_perm:[0,0,0,0] row_mask:0xf bank_mask:0xf\n\tv_and_b32_dpp v19, v30, v63 quad_perm:[0,0,0,0] row_mask:0xf bank_mask:0xf\n\tv_mfma_f32_16x16x32_bf16 v[48:51], a[4:7], v[20:23], v[48:51]\n\tv_and_b32_dpp v20, v31, v60 quad_perm:[0,0,0,0] row_mask:0xf bank_mask:0xf\n\tv_and_b32_dpp v21, v31, v61 quad_perm:[0,0,0,0] row_mask:0xf bank_mask:0xf\n\tv_and_b32_dpp v22, v31, v62 quad_perm:[0,0,0,0] row_mask:0xf bank_mask:0xf\n\tv_and_b32_dpp v23, v31, v63 quad_perm:[0,0,0,0] row_mask:0xf bank_mask:0xf\n\tv_mfma_f32_16x16x32_bf16 v[48:51], a[8:11], v[16:19], v[48:51]\n\tv_and_b32_dpp v16, v28, v60 quad_perm:[1,1,1,1] row_mask:0xf bank_mask:0xf\n\tv_and_b32_dpp v17, v28, v61 quad_perm:[1,1,1,1] row_mask:0xf bank_mask:0xf\n\tv_and_b32_dpp v18, v28, v62 quad_perm:[1,1,1,1] row_mask:0xf bank_mask:0xf\n\tv_and_b32_dpp v19, v28, v63 quad_perm:[1,1,1,1] row_mask:0xf bank_mask:0xf\n\tv_mfma_f32_16x16x32_bf16 v[48:51], a[12:15], v[20:23], v[48:51]\n\tv_and_b32_dpp v20, v29, v60 quad_perm:[1,1,1,1] row_mask:0xf bank_mask:0xf\n\tv_and_b32_dpp v21, v29, v61 quad_perm:[1,1,1,1] row_mask:0xf bank_mask:0xf\n\tv_and_b32_dpp v22, v29, v62 quad_perm:[1,1,1,1] row_mask:0xf bank_mask:0xf\n\tv_and_b32_dpp v23, v29, v63 quad_perm:[1,1,1,1] row_mask:0xf bank_mask:0xf\n\tv_mfma_f32_16x16x32_bf16 v[48:51], a[16:19], v[16:19], v[48:51]\n\tv_and_b32_dpp v16, v30, v60 quad_perm:[1,1,1,1] row_mask:0xf bank_mask:0xf\n\tv_and_b32_dpp v17, v30, v61 quad_perm:[1,1,1,1] row_mask:0xf bank_mask:0xf\n\tv_and_b32_dpp v18, v30, v62 quad_perm:[1,1,1,1] row_mask:0xf bank_mask:0xf\n\tv_and_b32_dpp v19, v30, v63 quad_perm:[1,1,1,1] row_mask:0xf bank_mask:0xf\n\tv_mfma_f32_16x16x32_bf16 v[48:51], a[20:23], v[20:23], v[48:51]\n\tv_and_b32_dpp v20, v31, v60 quad_perm:[1,1,1,1] row_mask:0xf bank_mask:0xf\n\tv_and_b32_dpp v21, v31, v61 quad_perm:[1,1,1,1] row_mask:0xf bank_mask:0xf\n\tv_and_b32_dpp v22, v31, v62 quad_perm:[1,1,1,1] row_mask:0xf bank_mask:0xf\n\tv_and_b32_dpp v23, v31, v63 quad_perm:[1,1,1,1] row_mask:0xf bank_mask:0xf\n\tv_mfma_f32_16x16x32_bf16 v[48:51], a[24:27], v[16:19], v[48:51]\n\tv_and_b32_dpp v16, v28, v60 quad_perm:[2,2,2,2] row_mask:0xf bank_mask:0xf\n\tv_and_b32_dpp v17, v28, v61 quad_perm:[2,2,2,2] row_mask:0xf bank_mask:0xf\n\tv_and_b32_dpp v18, v28, v62 quad_perm:[2,2,2,2] row_mask:0xf bank_mask:0xf\n\tv_and_b32_dpp v19, v28, v63 quad_perm:[2,2,2,2] row_mask:0xf bank_mask:0xf\n\tv_mfma_f32_16x16x32_bf16 v[48:51], a[28:31], v[20:23], v[48:51]\n\tv_and_b32_dpp v20, v29, v60 quad_perm:[2,2,2,2] row_mask:0xf bank_mask:0xf\n\tv_and_b32_dpp v21, v29, v61 quad_perm:[2,2,2,2] row_mask:0xf bank_mask:0xf\n\tv_and_b32_dpp v22, v29, v62 quad_perm:[2,2,2,2] row_mask:0xf bank_mask:0xf\n\tv_and_b32_dpp v23, v29, v63 quad_perm:[2,2,2,2] row_mask:0xf bank_mask:0xf\n\tv_mfma_f32_16x16x32_bf16 v[48:51], a[32:35], v[16:19], v[48:51]\n\tv_and_b32_dpp v16, v30, v60 quad_perm:[2,2,2,2] row_mask:0xf bank_mask:0xf\n\tv_and_b32_dpp v17, v30, v61 quad_perm:[2,2,2,2] row_mask:0xf bank_mask:0xf\n\tv_and_b32_dpp v18, v30, v62 quad_perm:[2,2,2,2] row_mask:0xf bank_mask:0xf\n\tv_and_b32_dpp v19, v30, v63 quad_perm:[2,2,2,2] row_mask:0xf bank_mask:0xf\n\tv_mfma_f32_16x16x32_bf16 v[48:51], a[36:39], v[20:23], v[48:51]\n\tv_and_b32_dpp v20, v31, v60 quad_perm:[2,2,2,2] row_mask:0xf bank_mask:0xf\n\tv_and_b32_dpp v21, v31, v61 quad_perm:[2,2,2,2] row_mask:0xf bank_mask:0xf\n\tv_and_b32_dpp v22, v31, v62 quad_perm:[2,2,2,2] row_mask:0xf bank_mask:0xf\n\tv_and_b32_dpp v23, v31, v63 quad_perm:[2,2,2,2] row_mask:0xf bank_mask:0xf\n\tv_mfma_f32_16x16x32_bf16 v[48:51], a[40:43], v[16:19], v[48:51]\n\tv_and_b32_dpp v16, v28, v60 quad_perm:[3,3,3,3] row_mask:0xf bank_mask:0xf\n\tv_and_b32_dpp v17, v28, v61 quad_perm:[3,3,3,3] row_mask:0xf bank_mask:0xf\n\tv_and_b32_dpp v18, v28, v62 quad_perm:[3,3,3,3] row_mask:0xf bank_mask:0xf\n\tv_and_b32_dpp v19, v28, v63 quad_perm:[3,3,3,3] row_mask:0xf bank_mask:0xf\n\tv_mfma_f32_16x16x32_bf16 v[48:51], a[44:47], v[20:23], v[48:51]\n\tv_and_b32_dpp v20, v29, v60 quad_perm:[3,3,3,3] row_mask:0xf bank_mask:0xf\n\tv_and_b32_dpp v21, v29, v61 quad_perm:[3,3,3,3] row_mask:0xf bank_mask:0xf\n\tv_and_b32_dpp v22, v29, v62 quad_perm:[3,3,3,3] row_mask:0xf bank_mask:0xf\n\tv_and_b32_dpp v23, v29, v63 quad_perm:[3,3,3,3] row_mask:0xf bank_mask:0xf\n\tv_mfma_f32_16x16x32_bf16 v[48:51], a[48:51], v[16:19], v[48:51]\n\tv_and_b32_dpp v16, v30, v60 quad_perm:[3,3,3,3] row_mask:0xf bank_mask:0xf\n\tv_and_b32_dpp v17, v30, v61 quad_perm:[3,3,3,3] row_mask:0xf bank_mask:0xf\n\tv_and_b32_dpp v18, v30, v62 quad_perm:[3,3,3,3] row_mask:0xf bank_mask:0xf\n\tv_and_b32_dpp v19, v30, v63 quad_perm:[3,3,3,3] row_mask:0xf bank_mask:0xf\n\tv_mfma_f32_16x16x32_bf16 v[48:51], a[52:55], v[20:23], v[48:51]\n\tv_and_b32_dpp v20, v31, v60 quad_perm:[3,3,3,3] row_mask:0xf bank_mask:0xf\n\tv_and_b32_dpp v21, v31, v61 quad_perm:[3,3,3,3] row_mask:0xf bank_mask:0xf\n\tv_and_b32_dpp v22, v31, v62 quad_perm:[3,3,3,3] row_mask:0xf bank_mask:0xf\n\tv_and_b32_dpp v23, v31, v63 quad_perm:[3,3,3,3] row_mask:0xf bank_mask:0xf\n\tv_mfma_f32_16x16x32_bf16 v[48:51], a[56:59], v[16:19], v[48:51]\n\tv_and_b32_dpp v16, v32, v60 quad_perm:[0,0,0,0] row_mask:0xf bank_mask:0xf\n\tv_and_b32_dpp v17, v32, v61 quad_perm:[0,0,0,0] row_mask:0xf bank_mask:0xf\n\tv_and_b32_dpp v18, v32, v62 quad_perm:[0,0,0,0] row_mask:0xf bank_mask:0xf\n\tv_and_b32_dpp v19, v32, v63 quad_perm:[0,0,0,0] row_mask:0xf bank_mask:0xf\n\tv_mfma_f32_16x16x32_bf16 v[48:51], a[60:63], v[20:23], v[48:51]\n\tv_and_b32_dpp v20, v33, v60 quad_perm:[0,0,0,0] row_mask:0xf bank_mask:0xf\n\tv_and_b32_dpp v21, v33, v61 quad_perm:[0,0,0,0] row_mask:0xf bank_mask:0xf\n\tv_and_b32_dpp v22, v33, v62 quad_perm:[0,0,0,0] row_mask:0xf bank_mask:0xf\n\tv_and_b32_dpp v23, v33, v63 quad_perm:[0,0,0,0] row_mask:0xf bank_mask:0xf\n\tv_mfma_f32_16x16x32_bf16 v[48:51], a[176:179], v[16:19], v[48:51]\n\tv_and_b32_dpp v16, v34, v60 quad_perm:[0,0,0,0] row_mask:0xf bank_mask:0xf\n\tv_and_b32_dpp v17, v34, v61 quad_perm:[0,0,0,0] row_mask:0xf bank_mask:0xf\n\tv_and_b32_dpp v18, v34, v62 quad_perm:[0,0,0,0] row_mask:0xf bank_mask:0xf\n\tv_and_b32_dpp v19, v34, v63 quad_perm:[0,0,0,0] row_mask:0xf bank_mask:0xf\n\tv_mfma_f32_16x16x32_bf16 v[48:51], a[180:183], v[20:23], v[48:51]\n\tv_and_b32_dpp v20, v35, v60 quad_perm:[0,0,0,0] row_mask:0xf bank_mask:0xf\n\tv_and_b32_dpp v21, v35, v61 quad_perm:[0,0,0,0] row_mask:0xf bank_mask:0xf\n\tv_and_b32_dpp v22, v35, v62 quad_perm:[0,0,0,0] row_mask:0xf bank_mask:0xf\n\tv_and_b32_dpp v23, v35, v63 quad_perm:[0,0,0,0] row_mask:0xf bank_mask:0xf\n\tv_mfma_f32_16x16x32_bf16 v[48:51], a[184:187], v[16:19], v[48:51]\n\tv_and_b32_dpp v16, v32, v60 quad_perm:[1,1,1,1] row_mask:0xf bank_mask:0xf\n\tv_and_b32_dpp v17, v32, v61 quad_perm:[1,1,1,1] row_mask:0xf bank_mask:0xf\n\tv_and_b32_dpp v18, v32, v62 quad_perm:[1,1,1,1] row_mask:0xf bank_mask:0xf\n\tv_and_b32_dpp v19, v32, v63 quad_perm:[1,1,1,1] row_mask:0xf bank_mask:0xf\n\tv_mfma_f32_16x16x32_bf16 v[48:51], a[188:191], v[20:23], v[48:51]\n\tv_and_b32_dpp v20, v33, v60 quad_perm:[1,1,1,1] row_mask:0xf bank_mask:0xf\n\tv_and_b32_dpp v21, v33, v61 quad_perm:[1,1,1,1] row_mask:0xf bank_mask:0xf\n\tv_and_b32_dpp v22, v33, v62 quad_perm:[1,1,1,1] row_mask:0xf bank_mask:0xf\n\tv_and_b32_dpp v23, v33, v63 quad_perm:[1,1,1,1] row_mask:0xf bank_mask:0xf\n\tv_mfma_f32_16x16x32_bf16 v[48:51], a[64:67], v[16:19], v[48:51]\n\tv_and_b32_dpp v16, v34, v60 quad_perm:[1,1,1,1] row_mask:0xf bank_mask:0xf\n\tv_and_b32_dpp v17, v34, v61 quad_perm:[1,1,1,1] row_mask:0xf bank_mask:0xf\n\tv_and_b32_dpp v18, v34, v62 quad_perm:[1,1,1,1] row_mask:0xf bank_mask:0xf\n\tv_and_b32_dpp v19, v34, v63 quad_perm:[1,1,1,1] row_mask:0xf bank_mask:0xf\n\tv_mfma_f32_16x16x32_bf16 v[48:51], a[68:71], v[20:23], v[48:51]\n\tv_and_b32_dpp v20, v35, v60 quad_perm:[1,1,1,1] row_mask:0xf bank_mask:0xf\n\tv_and_b32_dpp v21, v35, v61 quad_perm:[1,1,1,1] row_mask:0xf bank_mask:0xf\n\tv_and_b32_dpp v22, v35, v62 quad_perm:[1,1,1,1] row_mask:0xf bank_mask:0xf\n\tv_and_b32_dpp v23, v35, v63 quad_perm:[1,1,1,1] row_mask:0xf bank_mask:0xf\n\tv_mfma_f32_16x16x32_bf16 v[48:51], a[72:75], v[16:19], v[48:51]\n\tv_and_b32_dpp v16, v32, v60 quad_perm:[2,2,2,2] row_mask:0xf bank_mask:0xf\n\tv_and_b32_dpp v17, v32, v61 quad_perm:[2,2,2,2] row_mask:0xf bank_mask:0xf\n\tv_and_b32_dpp v18, v32, v62 quad_perm:[2,2,2,2] row_mask:0xf bank_mask:0xf\n\tv_and_b32_dpp v19, v32, v63 quad_perm:[2,2,2,2] row_mask:0xf bank_mask:0xf\n\tv_mfma_f32_16x16x32_bf16 v[48:51], a[76:79], v[20:23], v[48:51]\n\tv_xor_b32 v14, 0x4080000, v45\n\tglobal_load_dwordx4 v[96:99], v14, %3 offset:0 nt\n\tglobal_load_dwordx4 v[100:103], v14, %3 offset:256 nt\n\tglobal_load_dwordx4 v[104:107], v14, %3 offset:512 nt\n\tglobal_load_dwordx4 v[108:111], v14, %3 offset:768 nt\n\tv_and_b32_dpp v20, v33, v60 quad_perm:[2,2,2,2] row_mask:0xf bank_mask:0xf\n\tv_and_b32_dpp v21, v33, v61 quad_perm:[2,2,2,2] row_mask:0xf bank_mask:0xf\n\tv_and_b32_dpp v22, v33, v62 quad_perm:[2,2,2,2] row_mask:0xf bank_mask:0xf\n\tv_and_b32_dpp v23, v33, v63 quad_perm:[2,2,2,2] row_mask:0xf bank_mask:0xf\n\tv_mfma_f32_16x16x32_bf16 v[48:51], a[80:83], v[16:19], v[48:51]\n\tv_and_b32_dpp v16, v34, v60 quad_perm:[2,2,2,2] row_mask:0xf bank_mask:0xf\n\tv_and_b32_dpp v17, v34, v61 quad_perm:[2,2,2,2] row_mask:0xf bank_mask:0xf\n\tv_and_b32_dpp v18, v34, v62 quad_perm:[2,2,2,2] row_mask:0xf bank_mask:0xf\n\tv_and_b32_dpp v19, v34, v63 quad_perm:[2,2,2,2] row_mask:0xf bank_mask:0xf\n\tv_mfma_f32_16x16x32_bf16 v[48:51], a[84:87], v[20:23], v[48:51]\n\tv_and_b32_dpp v20, v35, v60 quad_perm:[2,2,2,2] row_mask:0xf bank_mask:0xf\n\tv_and_b32_dpp v21, v35, v61 quad_perm:[2,2,2,2] row_mask:0xf bank_mask:0xf\n\tv_and_b32_dpp v22, v35, v62 quad_perm:[2,2,2,2] row_mask:0xf bank_mask:0xf\n\tv_and_b32_dpp v23, v35, v63 quad_perm:[2,2,2,2] row_mask:0xf bank_mask:0xf\n\tv_mfma_f32_16x16x32_bf16 v[48:51], a[88:91], v[16:19], v[48:51]\n\tv_and_b32_dpp v16, v32, v60 quad_perm:[3,3,3,3] row_mask:0xf bank_mask:0xf\n\tv_and_b32_dpp v17, v32, v61 quad_perm:[3,3,3,3] row_mask:0xf bank_mask:0xf\n\tv_and_b32_dpp v18, v32, v62 quad_perm:[3,3,3,3] row_mask:0xf bank_mask:0xf\n\tv_and_b32_dpp v19, v32, v63 quad_perm:[3,3,3,3] row_mask:0xf bank_mask:0xf\n\tv_mfma_f32_16x16x32_bf16 v[48:51], a[92:95], v[20:23], v[48:51]\n\tv_and_b32_dpp v20, v33, v60 quad_perm:[3,3,3,3] row_mask:0xf bank_mask:0xf\n\tv_and_b32_dpp v21, v33, v61 quad_perm:[3,3,3,3] row_mask:0xf bank_mask:0xf\n\tv_and_b32_dpp v22, v33, v62 quad_perm:[3,3,3,3] row_mask:0xf bank_mask:0xf\n\tv_and_b32_dpp v23, v33, v63 quad_perm:[3,3,3,3] row_mask:0xf bank_mask:0xf\n\tv_mfma_f32_16x16x32_bf16 v[48:51], a[96:99], v[16:19], v[48:51]\n\tv_and_b32_dpp v16, v34, v60 quad_perm:[3,3,3,3] row_mask:0xf bank_mask:0xf\n\tv_and_b32_dpp v17, v34, v61 quad_perm:[3,3,3,3] row_mask:0xf bank_mask:0xf\n\tv_and_b32_dpp v18, v34, v62 quad_perm:[3,3,3,3] row_mask:0xf bank_mask:0xf\n\tv_and_b32_dpp v19, v34, v63 quad_perm:[3,3,3,3] row_mask:0xf bank_mask:0xf\n\tv_mfma_f32_16x16x32_bf16 v[48:51], a[100:103], v[20:23], v[48:51]\n\tv_and_b32_dpp v20, v35, v60 quad_perm:[3,3,3,3] row_mask:0xf bank_mask:0xf\n\tv_and_b32_dpp v21, v35, v61 quad_perm:[3,3,3,3] row_mask:0xf bank_mask:0xf\n\tv_and_b32_dpp v22, v35, v62 quad_perm:[3,3,3,3] row_mask:0xf bank_mask:0xf\n\tv_and_b32_dpp v23, v35, v63 quad_perm:[3,3,3,3] row_mask:0xf bank_mask:0xf\n\tv_mfma_f32_16x16x32_bf16 v[48:51], a[104:107], v[16:19], v[48:51]\n\tv_and_b32_dpp v16, v36, v60 quad_perm:[0,0,0,0] row_mask:0xf bank_mask:0xf\n\tv_and_b32_dpp v17, v36, v61 quad_perm:[0,0,0,0] row_mask:0xf bank_mask:0xf\n\tv_and_b32_dpp v18, v36, v62 quad_perm:[0,0,0,0] row_mask:0xf bank_mask:0xf\n\tv_and_b32_dpp v19, v36, v63 quad_perm:[0,0,0,0] row_mask:0xf bank_mask:0xf\n\tv_mfma_f32_16x16x32_bf16 v[48:51], a[108:111], v[20:23], v[48:51]\n\tv_and_b32_dpp v20, v37, v60 quad_perm:[0,0,0,0] row_mask:0xf bank_mask:0xf\n\tv_and_b32_dpp v21, v37, v61 quad_perm:[0,0,0,0] row_mask:0xf bank_mask:0xf\n\tv_and_b32_dpp v22, v37, v62 quad_perm:[0,0,0,0] row_mask:0xf bank_mask:0xf\n\tv_and_b32_dpp v23, v37, v63 quad_perm:[0,0,0,0] row_mask:0xf bank_mask:0xf\n\tv_mfma_f32_16x16x32_bf16 v[48:51], a[112:115], v[16:19], v[48:51]\n\tv_and_b32_dpp v16, v38, v60 quad_perm:[0,0,0,0] row_mask:0xf bank_mask:0xf\n\tv_and_b32_dpp v17, v38, v61 quad_perm:[0,0,0,0] row_mask:0xf bank_mask:0xf\n\tv_and_b32_dpp v18, v38, v62 quad_perm:[0,0,0,0] row_mask:0xf bank_mask:0xf\n\tv_and_b32_dpp v19, v38, v63 quad_perm:[0,0,0,0] row_mask:0xf bank_mask:0xf\n\tv_mfma_f32_16x16x32_bf16 v[48:51], a[116:119], v[20:23], v[48:51]\n\tv_and_b32_dpp v20, v39, v60 quad_perm:[0,0,0,0] row_mask:0xf bank_mask:0xf\n\tv_and_b32_dpp v21, v39, v61 quad_perm:[0,0,0,0] row_mask:0xf bank_mask:0xf\n\tv_and_b32_dpp v22, v39, v62 quad_perm:[0,0,0,0] row_mask:0xf bank_mask:0xf\n\tv_and_b32_dpp v23, v39, v63 quad_perm:[0,0,0,0] row_mask:0xf bank_mask:0xf\n\tv_mfma_f32_16x16x32_bf16 v[48:51], a[120:123], v[16:19], v[48:51]\n\tv_and_b32_dpp v16, v36, v60 quad_perm:[1,1,1,1] row_mask:0xf bank_mask:0xf\n\tv_and_b32_dpp v17, v36, v61 quad_perm:[1,1,1,1] row_mask:0xf bank_mask:0xf\n\tv_and_b32_dpp v18, v36, v62 quad_perm:[1,1,1,1] row_mask:0xf bank_mask:0xf\n\tv_and_b32_dpp v19, v36, v63 quad_perm:[1,1,1,1] row_mask:0xf bank_mask:0xf\n\tv_mfma_f32_16x16x32_bf16 v[48:51], a[124:127], v[20:23], v[48:51]\n\tv_and_b32_dpp v20, v37, v60 quad_perm:[1,1,1,1] row_mask:0xf bank_mask:0xf\n\tv_and_b32_dpp v21, v37, v61 quad_perm:[1,1,1,1] row_mask:0xf bank_mask:0xf\n\tv_and_b32_dpp v22, v37, v62 quad_perm:[1,1,1,1] row_mask:0xf bank_mask:0xf\n\tv_and_b32_dpp v23, v37, v63 quad_perm:[1,1,1,1] row_mask:0xf bank_mask:0xf\n\tv_mfma_f32_16x16x32_bf16 v[48:51], a[144:147], v[16:19], v[48:51]\n\tv_and_b32_dpp v16, v38, v60 quad_perm:[1,1,1,1] row_mask:0xf bank_mask:0xf\n\tv_and_b32_dpp v17, v38, v61 quad_perm:[1,1,1,1] row_mask:0xf bank_mask:0xf\n\tv_and_b32_dpp v18, v38, v62 quad_perm:[1,1,1,1] row_mask:0xf bank_mask:0xf\n\tv_and_b32_dpp v19, v38, v63 quad_perm:[1,1,1,1] row_mask:0xf bank_mask:0xf\n\tv_mfma_f32_16x16x32_bf16 v[48:51], a[148:151], v[20:23], v[48:51]\n\tv_and_b32_dpp v20, v39, v60 quad_perm:[1,1,1,1] row_mask:0xf bank_mask:0xf\n\tv_and_b32_dpp v21, v39, v61 quad_perm:[1,1,1,1] row_mask:0xf bank_mask:0xf\n\tv_and_b32_dpp v22, v39, v62 quad_perm:[1,1,1,1] row_mask:0xf bank_mask:0xf\n\tv_and_b32_dpp v23, v39, v63 quad_perm:[1,1,1,1] row_mask:0xf bank_mask:0xf\n\tv_mfma_f32_16x16x32_bf16 v[48:51], a[152:155], v[16:19], v[48:51]\n\tv_and_b32_dpp v16, v36, v60 quad_perm:[2,2,2,2] row_mask:0xf bank_mask:0xf\n\tv_and_b32_dpp v17, v36, v61 quad_perm:[2,2,2,2] row_mask:0xf bank_mask:0xf\n\tv_and_b32_dpp v18, v36, v62 quad_perm:[2,2,2,2] row_mask:0xf bank_mask:0xf\n\tv_and_b32_dpp v19, v36, v63 quad_perm:[2,2,2,2] row_mask:0xf bank_mask:0xf\n\tv_mfma_f32_16x16x32_bf16 v[48:51], a[156:159], v[20:23], v[48:51]\n\tv_and_b32_dpp v20, v37, v60 quad_perm:[2,2,2,2] row_mask:0xf bank_mask:0xf\n\tv_and_b32_dpp v21, v37, v61 quad_perm:[2,2,2,2] row_mask:0xf bank_mask:0xf\n\tv_and_b32_dpp v22, v37, v62 quad_perm:[2,2,2,2] row_mask:0xf bank_mask:0xf\n\tv_and_b32_dpp v23, v37, v63 quad_perm:[2,2,2,2] row_mask:0xf bank_mask:0xf\n\tv_mfma_f32_16x16x32_bf16 v[48:51], a[160:163], v[16:19], v[48:51]\n\tv_and_b32_dpp v16, v38, v60 quad_perm:[2,2,2,2] row_mask:0xf bank_mask:0xf\n\tv_and_b32_dpp v17, v38, v61 quad_perm:[2,2,2,2] row_mask:0xf bank_mask:0xf\n\tv_and_b32_dpp v18, v38, v62 quad_perm:[2,2,2,2] row_mask:0xf bank_mask:0xf\n\tv_and_b32_dpp v19, v38, v63 quad_perm:[2,2,2,2] row_mask:0xf bank_mask:0xf\n\tv_mfma_f32_16x16x32_bf16 v[48:51], a[164:167], v[20:23], v[48:51]\n\tv_and_b32_dpp v20, v39, v60 quad_perm:[2,2,2,2] row_mask:0xf bank_mask:0xf\n\tv_and_b32_dpp v21, v39, v61 quad_perm:[2,2,2,2] row_mask:0xf bank_mask:0xf\n\tv_and_b32_dpp v22, v39, v62 quad_perm:[2,2,2,2] row_mask:0xf bank_mask:0xf\n\tv_and_b32_dpp v23, v39, v63 quad_perm:[2,2,2,2] row_mask:0xf bank_mask:0xf\n\tv_mfma_f32_16x16x32_bf16 v[48:51], a[168:171], v[16:19], v[48:51]\n\tv_and_b32_dpp v16, v36, v60 quad_perm:[3,3,3,3] row_mask:0xf bank_mask:0xf\n\tv_and_b32_dpp v17, v36, v61 quad_perm:[3,3,3,3] row_mask:0xf bank_mask:0xf\n\tv_and_b32_dpp v18, v36, v62 quad_perm:[3,3,3,3] row_mask:0xf bank_mask:0xf\n\tv_and_b32_dpp v19, v36, v63 quad_perm:[3,3,3,3] row_mask:0xf bank_mask:0xf\n\tv_mfma_f32_16x16x32_bf16 v[48:51], a[172:175], v[20:23], v[48:51]\n\tv_and_b32_dpp v20, v37, v60 quad_perm:[3,3,3,3] row_mask:0xf bank_mask:0xf\n\tv_and_b32_dpp v21, v37, v61 quad_perm:[3,3,3,3] row_mask:0xf bank_mask:0xf\n\tv_and_b32_dpp v22, v37, v62 quad_perm:[3,3,3,3] row_mask:0xf bank_mask:0xf\n\tv_and_b32_dpp v23, v37, v63 quad_perm:[3,3,3,3] row_mask:0xf bank_mask:0xf\n\tv_mfma_f32_16x16x32_bf16 v[48:51], a[192:195], v[16:19], v[48:51]\n\tv_and_b32_dpp v16, v38, v60 quad_perm:[3,3,3,3] row_mask:0xf bank_mask:0xf\n\tv_and_b32_dpp v17, v38, v61 quad_perm:[3,3,3,3] row_mask:0xf bank_mask:0xf\n\tv_and_b32_dpp v18, v38, v62 quad_perm:[3,3,3,3] row_mask:0xf bank_mask:0xf\n\tv_and_b32_dpp v19, v38, v63 quad_perm:[3,3,3,3] row_mask:0xf bank_mask:0xf\n\tv_mfma_f32_16x16x32_bf16 v[48:51], a[196:199], v[20:23], v[48:51]\n\tv_and_b32_dpp v20, v39, v60 quad_perm:[3,3,3,3] row_mask:0xf bank_mask:0xf\n\tv_and_b32_dpp v21, v39, v61 quad_perm:[3,3,3,3] row_mask:0xf bank_mask:0xf\n\tv_and_b32_dpp v22, v39, v62 quad_perm:[3,3,3,3] row_mask:0xf bank_mask:0xf\n\tv_and_b32_dpp v23, v39, v63 quad_perm:[3,3,3,3] row_mask:0xf bank_mask:0xf\n\tv_mfma_f32_16x16x32_bf16 v[48:51], a[200:203], v[16:19], v[48:51]\n\ts_nop 0\n\tv_mfma_f32_16x16x32_bf16 v[48:51], a[204:207], v[20:23], v[48:51]\n\ts_nop 7\n\ts_nop 0\n\tds_write2_b32 v13, v48, v49 offset1:8\n\tds_write2_b32 v13, v50, v51 offset0:16 offset1:24" :  : "s"(CSC), "s"(Kb), "s"(lb), "s"(Vb), "s"(MRG + w*1024u), "s"(MLB + w*8u) : "memory","scc","vcc","s98","s99","v4","v5","v6","v7","v8","v9","v10","v11","v12","v13","v14","v15","v16","v17","v18","v19","v20","v21","v22","v23","v24","v25","v26","v27","v28","v29","v30","v31","v32","v33","v34","v35","v36","v37","v38","v39","v40","v41","v42","v43");
  pace(tb, (d16 * 4u) >> 4);
  asm volatile("v_xor_b32 v43, 0x40c0000, v45\n\tglobal_load_dwordx4 v[112:115], v43, %0 offset:0 nt\n\tglobal_load_dwordx4 v[116:119], v43, %0 offset:256 nt\n\tglobal_load_dwordx4 v[120:123], v43, %0 offset:512 nt\n\tglobal_load_dwordx4 v[124:127], v43, %0 offset:768 nt" :  : "s"(Vb) : "memory","scc","v43","v42","s98","s99");
  tref = merge_publish(w, s, wsb + 229376ull + (u64)kvh*10240u);
  asm volatile("v_mov_b32 v40, 7776\n\ts_mov_b32 vcc_lo, 0x11111111\n\ts_mov_b32 vcc_hi, 0x11111111\n\tv_cndmask_b32 v4, v40, v47, vcc\n\ts_lshl_b64 vcc, vcc, 1\n\tv_cndmask_b32 v5, v40, v47, vcc\n\ts_lshl_b64 vcc, vcc, 1\n\tv_cndmask_b32 v6, v40, v47, vcc\n\ts_lshl_b64 vcc, vcc, 1\n\tv_cndmask_b32 v7, v40, v47, vcc\n\tds_read_b128 v[16:19], v5 offset:8192\n\tds_read_b128 v[20:23], v5 offset:8208\n\tds_read_b128 v[8:11], v4 offset:8192\n\tds_read_b128 v[12:15], v4 offset:8208\n\tds_read_b128 v[24:27], v6 offset:8192\n\tds_read_b128 v[28:31], v6 offset:8208\n\tds_read_b128 v[32:35], v7 offset:8192\n\tds_read_b128 v[36:39], v7 offset:8208\n\ts_waitcnt vmcnt(16)\n\tds_read_b128 a[240:243], v46 offset:12288\n\tds_read_b128 a[248:251], v46 offset:14336\n\tds_read_b128 a[244:247], v46 offset:13312\n\tds_read_b128 a[252:255], v46 offset:15360\n\ts_waitcnt lgkmcnt(4)\n\tv_mfma_f32_16x16x32_bf16 v[4:7], v[240:243], v[16:19], 0\n\tv_mfma_f32_16x16x32_bf16 v[40:43], v[248:251], v[16:19], 0\n\tv_mfma_f32_16x16x32_bf16 v[4:7], v[244:247], v[20:23], v[4:7]\n\tv_mfma_f32_16x16x32_bf16 v[40:43], v[252:255], v[20:23], v[40:43]\n\ts_waitcnt lgkmcnt(2)\n\tv_mfma_f32_16x16x32_bf16 v[4:7], a[240:243], v[8:11], v[4:7]\n\tv_mfma_f32_16x16x32_bf16 v[40:43], a[248:251], v[8:11], v[40:43]\n\tds_read_b128 a[240:243], v46 offset:16384\n\tds_read_b128 a[248:251], v46 offset:18432\n\ts_waitcnt lgkmcnt(2)\n\tv_mfma_f32_16x16x32_bf16 v[4:7], a[244:247], v[12:15], v[4:7]\n\tv_mfma_f32_16x16x32_bf16 v[40:43], a[252:255], v[12:15], v[40:43]\n\tds_read_b128 a[244:247], v46 offset:17408\n\tds_read_b128 a[252:255], v46 offset:19456\n\ts_waitcnt lgkmcnt(2)\n\tv_mfma_f32_16x16x32_bf16 v[4:7], a[240:243], v[24:27], v[4:7]\n\tv_mfma_f32_16x16x32_bf16 v[40:43], a[248:251], v[24:27], v[40:43]\n\tds_read_b128 a[240:243], v46 offset:20480\n\tds_read_b128 a[248:251], v46 offset:22528\n\ts_waitcnt lgkmcnt(2)\n\tv_mfma_f32_16x16x32_bf16 v[4:7], a[244:247], v[28:31], v[4:7]\n\tv_mfma_f32_16x16x32_bf16 v[40:43], a[252:255], v[28:31], v[40:43]\n\tds_read_b128 a[244:247], v46 offset:21504\n\tds_read_b128 a[252:255], v46 offset:23552\n\ts_waitcnt lgkmcnt(2)\n\tv_mfma_f32_16x16x32_bf16 v[4:7], a[240:243], v[32:35], v[4:7]\n\tv_mfma_f32_16x16x32_bf16 v[40:43], a[248:251], v[32:35], v[40:43]\n\ts_waitcnt lgkmcnt(0)\n\tv_mfma_f32_16x16x32_bf16 v[4:7], a[244:247], v[36:39], v[4:7]\n\tv_mfma_f32_16x16x32_bf16 v[40:43], a[252:255], v[36:39], v[40:43]\n\ts_nop 7\n\ts_nop 0\n\tv_add_f32_dpp v8, v5, v4 row_ror:8 row_mask:0xf bank_mask:0x3\n\tv_add_f32_dpp v9, v7, v6 row_ror:8 row_mask:0xf bank_mask:0x3\n\tv_add_f32_dpp v10, v41, v40 row_ror:8 row_mask:0xf bank_mask:0x3\n\tv_add_f32_dpp v11, v43, v42 row_ror:8 row_mask:0xf bank_mask:0x3\n\tv_add_f32_dpp v8, v4, v5 row_ror:8 row_mask:0xf bank_mask:0xc\n\tv_add_f32_dpp v9, v6, v7 row_ror:8 row_mask:0xf bank_mask:0xc\n\tv_add_f32_dpp v10, v40, v41 row_ror:8 row_mask:0xf bank_mask:0xc\n\tv_add_f32_dpp v11, v42, v43 row_ror:8 row_mask:0xf bank_mask:0xc\n\tv_max3_f32 v12, v8, v9, v10\n\tv_max_f32 v12, v12, v11\n\ts_nop 1\n\tv_max_f32_dpp v12, v12, v12 quad_perm:[1,0,3,2] row_mask:0xf bank_mask:0xf\n\ts_nop 1\n\tv_max_f32_dpp v12, v12, v12 quad_perm:[2,3,0,1] row_mask:0xf bank_mask:0xf\n\tv_mov_b32 v13, v12\n\ts_nop 1\n\tv_permlane16_swap_b32 v12, v13\n\ts_nop 0\n\tv_max_f32 v12, v12, v13\n\tv_mov_b32 v13, v12\n\ts_nop 1\n\tv_permlane32_swap_b32 v12, v13\n\ts_nop 0\n\tv_max_f32 v12, v12, v13\n\tv_mul_f32 v15, %0, v12\n\tv_fma_f32 v8, v8, %0, -v15\n\tv_fma_f32 v9, v9, %0, -v15\n\tv_fma_f32 v10, v10, %0, -v15\n\tv_fma_f32 v11, v11, %0, -v15\n\tv_exp_f32 v8, v8\n\tv_exp_f32 v9, v9\n\tv_exp_f32 v10, v10\n\tv_exp_f32 v11, v11\n\tv_mov_b32 v56, v12\n\tv_cvt_pk_bf16_f32 v4, v8, v8\n\tv_cvt_pk_bf16_f32 v5, v9, v9\n\tv_cvt_pk_bf16_f32 v6, v10, v10\n\tv_cvt_pk_bf16_f32 v7, v11, v11\n\tv_add_f32 v8, v8, v9\n\tv_add_f32 v10, v10, v11\n\tv_add_f32 v57, v8, v10\n\tv_and_b32_dpp v16, v4, v60 quad_perm:[0,0,0,0] row_mask:0xf bank_mask:0xf\n\tv_and_b32_dpp v17, v4, v61 quad_perm:[0,0,0,0] row_mask:0xf bank_mask:0xf\n\tv_and_b32_dpp v18, v4, v62 quad_perm:[0,0,0,0] row_mask:0xf bank_mask:0xf\n\tv_and_b32_dpp v19, v4, v63 quad_perm:[0,0,0,0] row_mask:0xf bank_mask:0xf\n\tv_and_b32_dpp v20, v5, v60 quad_perm:[0,0,0,0] row_mask:0xf bank_mask:0xf\n\tv_and_b32_dpp v21, v5, v61 quad_perm:[0,0,0,0] row_mask:0xf bank_mask:0xf\n\tv_and_b32_dpp v22, v5, v62 quad_perm:[0,0,0,0] row_mask:0xf bank_mask:0xf\n\tv_and_b32_dpp v23, v5, v63 quad_perm:[0,0,0,0] row_mask:0xf bank_mask:0xf\n\t1:\n\ts_memrealtime s[98:99]\n\ts_waitcnt lgkmcnt(0)\n\ts_sub_u32 s98, s98, %1\n\ts_mul_i32 s99, %2, 5\n\ts_lshl_b32 s98, s98, 4\n\ts_cmp_lt_u32 s98, s99\n\ts_cbranch_scc1 1b\n\tv_xor_b32 v14, 0x4100000, v44\n\ts_add_u32 m0, %4, 24576\n\ts_nop 0\n\tglobal_load_lds_dwordx4 v14, %3 offset:0 nt\n\ts_add_u32 m0, %4, 25472\n\ts_nop 0\n\tglobal_load_lds_dwordx4 v14, %3 offset:128 nt\n\ts_add_u32 m0, %4, 26112\n\ts_nop 0\n\tglobal_load_lds_dwordx4 v14, %3 offset:512 nt\n\ts_add_u32 m0, %4, 27008\n\ts_nop 0\n\tglobal_load_lds_dwordx4 v14, %3 offset:640 nt\n\ts_waitcnt vmcnt(16)\n\tv_mfma_f32_16x16x32_bf16 v[48:51], a[128:131], v[16:19], 0\n\tv_and_b32_dpp v16, v6, v60 quad_perm:[0,0,0,0] row_mask:0xf bank_mask:0xf\n\tv_and_b32_dpp v17, v6, v61 quad_perm:[0,0,0,0] row_mask:0xf bank_mask:0xf\n\tv_and_b32_dpp v18, v6, v62 quad_perm:[0,0,0,0] row_mask:0xf bank_mask:0xf\n\tv_and_b32_dpp v19, v6, v63 quad_perm:[0,0,0,0] row_mask:0xf bank_mask:0xf\n\tv_mfma_f32_16x16x32_bf16 v[48:51], a[132:135], v[20:23], v[48:51]\n\tv_and_b32_dpp v20, v7, v60 quad_perm:[0,0,0,0] row_mask:0xf bank_mask:0xf\n\tv_and_b32_dpp v21, v7, v61 quad_perm:[0,0,0,0] row_mask:0xf bank_mask:0xf\n\tv_and_b32_dpp v22, v7, v62 quad_perm:[0,0,0,0] row_mask:0xf bank_mask:0xf\n\tv_and_b32_dpp v23, v7, v63 quad_perm:[0,0,0,0] row_mask:0xf bank_mask:0xf\n\tv_mfma_f32_16x16x32_bf16 v[48:51], a[136:139], v[16:19], v[48:51]\n\tv_and_b32_dpp v16, v4, v60 quad_perm:[1,1,1,1] row_mask:0xf bank_mask:0xf\n\tv_and_b32_dpp v17, v4, v61 quad_perm:[1,1,1,1] row_mask:0xf bank_mask:0xf\n\tv_and_b32_dpp v18, v4, v62 quad_perm:[1,1,1,1] row_mask:0xf bank_mask:0xf\n\tv_and_b32_dpp v19, v4, v63 quad_perm:[1,1,1,1] row_mask:0xf bank_mask:0xf\n\tv_mfma_f32_16x16x32_bf16 v[48:51], a[140:143], v[20:23], v[48:51]\n\tv_and_b32_dpp v20, v5, v60 quad_perm:[1,1,1,1] row_mask:0xf bank_mask:0xf\n\tv_and_b32_dpp v21, v5, v61 quad_perm:[1,1,1,1] row_mask:0xf bank_mask:0xf\n\tv_and_b32_dpp v22, v5, v62 quad_perm:[1,1,1,1] row_mask:0xf bank_mask:0xf\n\tv_and_b32_dpp v23, v5, v63 quad_perm:[1,1,1,1] row_mask:0xf bank_mask:0xf\n\t1:\n\ts_memrealtime s[98:99]\n\ts_waitcnt lgkmcnt(0)\n\ts_sub_u32 s98, s98, %1\n\ts_mul_i32 s99, %2, 6\n\ts_lshl_b32 s98, s98, 4\n\ts_cmp_lt_u32 s98, s99\n\ts_cbranch_scc1 1b\n\tv_xor_b32 v14, 0x4140000, v44\n\ts_add_u32 m0, %4, 28672\n\ts_nop 0\n\tglobal_load_lds_dwordx4 v14, %3 offset:0 nt\n\ts_add_u32 m0, %4, 29568\n\ts_nop 0\n\tglobal_load_lds_dwordx4 v14, %3 offset:128 nt\n\ts_add_u32 m0, %4, 30208\n\ts_nop 0\n\tglobal_load_lds_dwordx4 v14, %3 offset:512 nt\n\ts_add_u32 m0, %4, 31104\n\ts_nop 0\n\tglobal_load_lds_dwordx4 v14, %3 offset:640 nt\n\ts_waitcnt vmcnt(16)\n\tv_mfma_f32_16x16x32_bf16 v[48:51], v[80:83], v[16:19], v[48:51]\n\tv_and_b32_dpp v16, v6, v60 quad_perm:[1,1,1,1] row_mask:0xf bank_mask:0xf\n\tv_and_b32_dpp v17, v6, v61 quad_perm:[1,1,1,1] row_mask:0xf bank_mask:0xf\n\tv_and_b32_dpp v18, v6, v62 quad_perm:[1,1,1,1] row_mask:0xf bank_mask:0xf\n\tv_and_b32_dpp v19, v6, v63 quad_perm:[1,1,1,1] row_mask:0xf bank_mask:0xf\n\tv_mfma_f32_16x16x32_bf16 v[48:51], v[84:87], v[20:23], v[48:51]\n\tv_and_b32_dpp v20, v7, v60 quad_perm:[1,1,1,1] row_mask:0xf bank_mask:0xf\n\tv_and_b32_dpp v21, v7, v61 quad_perm:[1,1,1,1] row_mask:0xf bank_mask:0xf\n\tv_and_b32_dpp v22, v7, v62 quad_perm:[1,1,1,1] row_mask:0xf bank_mask:0xf\n\tv_and_b32_dpp v23, v7, v63 quad_perm:[1,1,1,1] row_mask:0xf bank_mask:0xf\n\tv_mfma_f32_16x16x32_bf16 v[48:51], v[88:91], v[16:19], v[48:51]\n\tv_and_b32_dpp v16, v4, v60 quad_perm:[2,2,2,2] row_mask:0xf bank_mask:0xf\n\tv_and_b32_dpp v17, v4, v61 quad_perm:[2,2,2,2] row_mask:0xf bank_mask:0xf\n\tv_and_b32_dpp v18, v4, v62 quad_perm:[2,2,2,2] row_mask:0xf bank_mask:0xf\n\tv_and_b32_dpp v19, v4, v63 quad_perm:[2,2,2,2] row_mask:0xf bank_mask:0xf\n\tv_mfma_f32_16x16x32_bf16 v[48:51], v[92:95], v[20:23], v[48:51]\n\tv_and_b32_dpp v20, v5, v60 quad_perm:[2,2,2,2] row_mask:0xf bank_mask:0xf\n\tv_and_b32_dpp v21, v5, v61 quad_perm:[2,2,2,2] row_mask:0xf bank_mask:0xf\n\tv_and_b32_dpp v22, v5, v62 quad_perm:[2,2,2,2] row_mask:0xf bank_mask:0xf\n\tv_and_b32_dpp v23, v5, v63 quad_perm:[2,2,2,2] row_mask:0xf bank_mask:0xf\n\t1:\n\ts_memrealtime s[98:99]\n\ts_waitcnt lgkmcnt(0)\n\ts_sub_u32 s98, s98, %1\n\ts_mul_i32 s99, %2, 7\n\ts_lshl_b32 s98, s98, 4\n\ts_cmp_lt_u32 s98, s99\n\ts_cbranch_scc1 1b\n\tv_xor_b32 v14, 0x4180000, v44\n\ts_add_u32 m0, %4, 32768\n\ts_nop 0\n\tglobal_load_lds_dwordx4 v14, %3 offset:0 nt\n\ts_add_u32 m0, %4, 33664\n\ts_nop 0\n\tglobal_load_lds_dwordx4 v14, %3 offset:128 nt\n\ts_add_u32 m0, %4, 34304\n\ts_nop 0\n\tglobal_load_lds_dwordx4 v14, %3 offset:512 nt\n\ts_add_u32 m0, %4, 35200\n\ts_nop 0\n\tglobal_load_lds_dwordx4 v14, %3 offset:640 nt\n\ts_waitcnt vmcnt(16)\n\tv_mfma_f32_16x16x32_bf16 v[48:51], v[96:99], v[16:19], v[48:51]\n\tv_and_b32_dpp v16, v6, v60 quad_perm:[2,2,2,2] row_mask:0xf bank_mask:0xf\n\tv_and_b32_dpp v17, v6, v61 quad_perm:[2,2,2,2] row_mask:0xf bank_mask:0xf\n\tv_and_b32_dpp v18, v6, v62 quad_perm:[2,2,2,2] row_mask:0xf bank_mask:0xf\n\tv_and_b32_dpp v19, v6, v63 quad_perm:[2,2,2,2] row_mask:0xf bank_mask:0xf\n\tv_mfma_f32_16x16x32_bf16 v[48:51], v[100:103], v[20:23], v[48:51]\n\tv_and_b32_dpp v20, v7, v60 quad_perm:[2,2,2,2] row_mask:0xf bank_mask:0xf\n\tv_and_b32_dpp v21, v7, v61 quad_perm:[2,2,2,2] row_mask:0xf bank_mask:0xf\n\tv_and_b32_dpp v22, v7, v62 quad_perm:[2,2,2,2] row_mask:0xf bank_mask:0xf\n\tv_and_b32_dpp v23, v7, v63 quad_perm:[2,2,2,2] row_mask:0xf bank_mask:0xf\n\tv_mfma_f32_16x16x32_bf16 v[48:51], v[104:107], v[16:19], v[48:51]\n\tv_and_b32_dpp v16, v4, v60 quad_perm:[3,3,3,3] row_mask:0xf bank_mask:0xf\n\tv_and_b32_dpp v17, v4, v61 quad_perm:[3,3,3,3] row_mask:0xf bank_mask:0xf\n\tv_and_b32_dpp v18, v4, v62 quad_perm:[3,3,3,3] row_mask:0xf bank_mask:0xf\n\tv_and_b32_dpp v19, v4, v63 quad_perm:[3,3,3,3] row_mask:0xf bank_mask:0xf\n\tv_mfma_f32_16x16x32_bf16 v[48:51], v[108:111], v[20:23], v[48:51]\n\tv_and_b32_dpp v20, v5, v60 quad_perm:[3,3,3,3] row_mask:0xf bank_mask:0xf\n\tv_and_b32_dpp v21, v5, v61 quad_perm:[3,3,3,3] row_mask:0xf bank_mask:0xf\n\tv_and_b32_dpp v22, v5, v62 quad_perm:[3,3,3,3] row_mask:0xf bank_mask:0xf\n\tv_and_b32_dpp v23, v5, v63 quad_perm:[3,3,3,3] row_mask:0xf bank_mask:0xf\n\t1:\n\ts_memrealtime s[98:99]\n\ts_waitcnt lgkmcnt(0)\n\ts_sub_u32 s98, s98, %1\n\ts_mul_i32 s99, %2, 8\n\ts_lshl_b32 s98, s98, 4\n\ts_cmp_lt_u32 s98, s99\n\ts_cbranch_scc1 1b\n\tv_xor_b32 v14, 0x41c0000, v44\n\ts_add_u32 m0, %4, 0\n\ts_nop 0\n\tglobal_load_lds_dwordx4 v14, %3 offset:0 nt\n\ts_add_u32 m0, %4, 896\n\ts_nop 0\n\tglobal_load_lds_dwordx4 v14, %3 offset:128 nt\n\ts_add_u32 m0, %4, 1536\n\ts_nop 0\n\tglobal_load_lds_dwordx4 v14, %3 offset:512 nt\n\ts_add_u32 m0, %4, 2432\n\ts_nop 0\n\tglobal_load_lds_dwordx4 v14, %3 offset:640 nt\n\ts_waitcnt vmcnt(16)\n\tv_mfma_f32_16x16x32_bf16 v[48:51], v[112:115], v[16:19], v[48:51]\n\tv_and_b32_dpp v16, v6, v60 quad_perm:[3,3,3,3] row_mask:0xf bank_mask:0xf\n\tv_and_b32_dpp v17, v6, v61 quad_perm:[3,3,3,3] row_mask:0xf bank_mask:0xf\n\tv_and_b32_dpp v18, v6, v62 quad_perm:[3,3,3,3] row_mask:0xf bank_mask:0xf\n\tv_and_b32_dpp v19, v6, v63 quad_perm:[3,3,3,3] row_mask:0xf bank_mask:0xf\n\tv_mfma_f32_16x16x32_bf16 v[48:51], v[116:119], v[20:23], v[48:51]\n\tv_and_b32_dpp v20, v7, v60 quad_perm:[3,3,3,3] row_mask:0xf bank_mask:0xf\n\tv_and_b32_dpp v21, v7, v61 quad_perm:[3,3,3,3] row_mask:0xf bank_mask:0xf\n\tv_and_b32_dpp v22, v7, v62 quad_perm:[3,3,3,3] row_mask:0xf bank_mask:0xf\n\tv_and_b32_dpp v23, v7, v63 quad_perm:[3,3,3,3] row_mask:0xf bank_mask:0xf\n\tv_mfma_f32_16x16x32_bf16 v[48:51], v[120:123], v[16:19], v[48:51]\n\ts_nop 0\n\tv_mfma_f32_16x16x32_bf16 v[48:51], v[124:127], v[20:23], v[48:51]" :  : "s"(CSC), "s"(tb), "s"(d16), "s"(Kb), "s"(lb) : "memory","scc","vcc","s98","s99","v4","v5","v6","v7","v8","v9","v10","v11","v12","v13","v14","v15","v16","v17","v18","v19","v20","v21","v22","v23","v24","v25","v26","v27","v28","v29","v30","v31","v32","v33","v34","v35","v36","v37","v38","v39","v40","v41","v42","v43");
  {
  u32 ln = LANE(); u32 po = (2*xg + (w>>1))*10240u + (w&1)*4096u + gpos(ln>>4, ln&15u); u32 pm = (2*xg + (w>>1))*10240u + 8192u + (ln&15u)*16u + (w&1)*8u;
  u64 pb = wsb + 229376ull; u32x4 a0,a1,a2,a3; u32x2 ml;
  u32 bpa = ln & 48u, rda = RED + ln*16u - (ln>>5)*256u + w*4u; u64 yd = pin(wsb + 741376ull + (u64)(xg*2560 + i32*80));
  asm volatile("" : "+v"(po), "+v"(pm), "+v"(bpa), "+v"(rda));
  asm volatile("global_load_dwordx4 %0, %5, %7 offset:0 \n\tglobal_load_dwordx4 %1, %5, %7 offset:1024 \n\tglobal_load_dwordx4 %2, %5, %7 offset:2048 \n\tglobal_load_dwordx4 %3, %5, %7 offset:3072 \n\tglobal_load_dwordx2 %4, %6, %7 \n\t1:\n\ts_memrealtime s[98:99]\n\ts_waitcnt lgkmcnt(0)\n\ts_sub_u32 s98, s98, %8\n\ts_mul_i32 s99, %9, 9\n\ts_lshl_b32 s98, s98, 4\n\ts_cmp_lt_u32 s98, s99\n\ts_cbranch_scc1 1b\n\tv_xor_b32 v43, 0x4100000, v45\n\tglobal_load_dwordx4 v[192:195], v43, %10 offset:0 nt\n\tglobal_load_dwordx4 v[196:199], v43, %10 offset:256 nt\n\tglobal_load_dwordx4 v[200:203], v43, %10 offset:512 nt\n\tglobal_load_dwordx4 v[204:207], v43, %10 offset:768 nt\n\t1:\n\ts_memrealtime s[98:99]\n\ts_waitcnt lgkmcnt(0)\n\ts_sub_u32 s98, s98, %8\n\ts_mul_i32 s99, %9, 10\n\ts_lshl_b32 s98, s98, 4\n\ts_cmp_lt_u32 s98, s99\n\ts_cbranch_scc1 1b\n\tv_xor_b32 v43, 0x4140000, v45\n\tglobal_load_dwordx4 v[208:211], v43, %10 offset:0 nt\n\tglobal_load_dwordx4 v[212:215], v43, %10 offset:256 nt\n\tglobal_load_dwordx4 v[216:219], v43, %10 offset:512 nt\n\tglobal_load_dwordx4 v[220:223], v43, %10 offset:768 nt\n\t1:\n\ts_memrealtime s[98:99]\n\ts_waitcnt lgkmcnt(0)\n\ts_sub_u32 s98, s98, %8\n\ts_mul_i32 s99, %9, 11\n\ts_lshl_b32 s98, s98, 4\n\ts_cmp_lt_u32 s98, s99\n\ts_cbranch_scc1 1b\n\tv_xor_b32 v43, 0x4180000, v45\n\tglobal_load_dwordx4 v[224:227], v43, %10 offset:0 nt\n\tglobal_load_dwordx4 v[228:231], v43, %10 offset:256 nt\n\tglobal_load_dwordx4 v[232:235], v43, %10 offset:512 nt\n\tglobal_load_dwordx4 v[236:239], v43, %10 offset:768 nt\n\ts_waitcnt vmcnt(12)" : "=&v"(a0),"=&v"(a1),"=&v"(a2),"=&v"(a3),"=&v"(ml) : "v"(po), "v"(pm), "s"(pb), "s"(tb), "s"(d16), "s"(Vb) : "memory","scc","v43","v42","s98","s99");
  _Pragma("unroll 1") for (int sp = 0; sp < SPIN; sp++) { u32 mx = umax3(umax4(a0), umax4(a1), umax3(umax4(a2), umax4(a3), max(ml[0], ml[1]))); if (!__any(mx == 0xFFFFFFFFu)) break; __builtin_amdgcn_s_sleep(1);
  asm volatile("global_load_dwordx4 %0, %5, %7 offset:0 sc1\n\tglobal_load_dwordx4 %1, %5, %7 offset:1024 sc1\n\tglobal_load_dwordx4 %2, %5, %7 offset:2048 sc1\n\tglobal_load_dwordx4 %3, %5, %7 offset:3072 sc1\n\tglobal_load_dwordx2 %4, %6, %7 sc1\n\ts_waitcnt vmcnt(0)" : "=&v"(a0),"=&v"(a1),"=&v"(a2),"=&v"(a3),"=&v"(ml) : "v"(po), "v"(pm), "s"(pb) : "memory","scc","v43","v42","s98","s99");
  }
  u32 t0_, t1_, t2_, t3_, t4_, t5_;
  asm volatile("s_mov_b32 vcc_lo, 0x30003\n\ts_mov_b32 vcc_hi, 0x30003\n\tv_max_f32_dpp %0, %10, %10 row_ror:8 row_mask:0xf bank_mask:0xf\n\tv_mfma_f32_16x16x32_bf16 v[28:31], %6, a[208:211], 0\n\tv_cndmask_b32 v36, 0, v52, vcc\n\tv_cndmask_b32 v37, 0, v53, vcc\n\tv_max_f32_dpp %0, %0, %0 row_ror:4 row_mask:0xf bank_mask:0xf\n\tv_cndmask_b32 v38, 0, v54, vcc\n\tv_cndmask_b32 v39, 0, v55, vcc\n\ts_lshl_b64 vcc, vcc, 2\n\tv_max_f32_dpp %0, %0, %0 row_ror:2 row_mask:0xf bank_mask:0xf\n\tv_cndmask_b32 v40, 0, v52, vcc\n\tv_cndmask_b32 v41, 0, v53, vcc\n\tv_max_f32_dpp %0, %0, %0 row_ror:1 row_mask:0xf bank_mask:0xf\n\tv_mfma_f32_16x16x32_bf16 v[32:35], %6, v[36:39], 0\n\tv_sub_f32 %0, %10, %0\n\tv_cndmask_b32 v42, 0, v54, vcc\n\tv_exp_f32 %0, %0\n\tv_cndmask_b32 v43, 0, v55, vcc\n\ts_lshl_b64 vcc, vcc, 2\n\tv_mfma_f32_16x16x32_bf16 v[28:31], %7, a[212:215], v[28:31]\n\tv_mul_f32 %0, %0, %11\n\tv_cndmask_b32 v36, 0, v52, vcc\n\tds_bpermute_b32 %2, %12, %0 offset:0\n\tds_bpermute_b32 %3, %12, %0 offset:4\n\tds_bpermute_b32 %4, %12, %0 offset:8\n\tds_bpermute_b32 %5, %12, %0 offset:12\n\tv_mfma_f32_16x16x32_bf16 v[32:35], %7, v[40:43], v[32:35]\n\tv_add_f32_dpp %1, %0, %0 row_ror:8 row_mask:0xf bank_mask:0xf\n\tv_cndmask_b32 v37, 0, v53, vcc\n\tv_cndmask_b32 v38, 0, v54, vcc\n\tv_add_f32_dpp %1, %1, %1 row_ror:4 row_mask:0xf bank_mask:0xf\n\tv_cndmask_b32 v39, 0, v55, vcc\n\ts_lshl_b64 vcc, vcc, 2\n\tv_mfma_f32_16x16x32_bf16 v[28:31], %8, a[216:219], v[28:31]\n\tv_add_f32_dpp %1, %1, %1 row_ror:2 row_mask:0xf bank_mask:0xf\n\tv_cndmask_b32 v40, 0, v52, vcc\n\tv_mfma_f32_16x16x32_bf16 v[32:35], %8, v[36:39], v[32:35]\n\tv_add_f32_dpp %1, %1, %1 row_ror:1 row_mask:0xf bank_mask:0xf\n\tv_cndmask_b32 v41, 0, v53, vcc\n\tv_cndmask_b32 v42, 0, v54, vcc\n\tv_cndmask_b32 v43, 0, v55, vcc\n\tv_mfma_f32_16x16x32_bf16 v[28:31], %9, a[220:223], v[28:31]\n\tv_rcp_f32 %1, %1\n\tv_mfma_f32_16x16x32_bf16 v[32:35], %9, v[40:43], v[32:35]\n\ts_nop 5\n\ts_waitcnt lgkmcnt(0)\n\tv_mul_f32 %0, %2, v28\n\tv_fmac_f32 %0, %3, v29\n\tv_fmac_f32 %0, %4, v30\n\tv_fmac_f32 %0, %5, v31\n\tv_mul_f32 %2, %2, v32\n\tv_fmac_f32 %2, %3, v33\n\tv_fmac_f32 %2, %4, v34\n\tv_fmac_f32 %2, %5, v35\n\ts_nop 1\n\tv_permlane32_swap_b32 %0, %2\n\ts_nop 0\n\tv_add_f32 %0, %0, %2\n\tv_mov_b32 %2, %0\n\ts_nop 1\n\tv_permlane16_swap_b32 %0, %2\n\ts_nop 0\n\tv_add_f32 %0, %0, %2\n\ts_nop 1\n\tv_add_f32_dpp %0, %0, %0 row_shl:2 row_mask:0x4 bank_mask:0xf\n\ts_nop 1\n\tv_add_f32_dpp %0, %0, %0 row_shl:4 row_mask:0x4 bank_mask:0xf\n\ts_mov_b32 exec_lo, 0xffff\n\ts_mov_b32 exec_hi, 3\n\tv_mul_f32 %0, %0, %1\n\tds_write_b32 %13, %0\n\ts_mov_b64 exec, -1\n\ts_waitcnt lgkmcnt(0)\n\ts_barrier" : "=&v"(t0_),"=&v"(t1_),"=&v"(t2_),"=&v"(t3_),"=&v"(t4_),"=&v"(t5_) : "v"(a0),"v"(a1),"v"(a2),"v"(a3),"v"(ml[0]),"v"(ml[1]),"v"(bpa),"v"(rda) : "memory","scc","vcc","v28","v29","v30","v31","v32","v33","v34","v35","v36","v37","v38","v39","v40","v41","v42","v43");
  tref = now32(); wo_finish(ln, w, yd); }
  pace(tb, (d16 * 12u) >> 4);
  asm volatile("v_xor_b32 v43, 0x41c0000, v45\n\tglobal_load_dwordx4 v[64:67], v43, %0 offset:0 nt\n\tglobal_load_dwordx4 v[68:71], v43, %0 offset:256 nt\n\tglobal_load_dwordx4 v[72:75], v43, %0 offset:512 nt\n\tglobal_load_dwordx4 v[76:79], v43, %0 offset:768 nt" :  : "s"(Vb) : "memory","scc","v43","v42","s98","s99");
  asm volatile("v_mov_b32 v40, 7776\n\ts_mov_b32 vcc_lo, 0x11111111\n\ts_mov_b32 vcc_hi, 0x11111111\n\tv_cndmask_b32 v4, v40, v47, vcc\n\ts_lshl_b64 vcc, vcc, 1\n\tv_cndmask_b32 v5, v40, v47, vcc\n\ts_lshl_b64 vcc, vcc, 1\n\tv_cndmask_b32 v6, v40, v47, vcc\n\ts_lshl_b64 vcc, vcc, 1\n\tv_cndmask_b32 v7, v40, v47, vcc\n\tds_read_b128 v[8:11], v4 offset:8192\n\tds_read_b128 v[12:15], v4 offset:8208\n\tds_read_b128 v[16:19], v5 offset:8192\n\tds_read_b128 v[20:23], v5 offset:8208\n\tds_read_b128 v[24:27], v6 offset:8192\n\tds_read_b128 v[28:31], v6 offset:8208\n\tds_read_b128 v[32:35], v7 offset:8192\n\tds_read_b128 v[36:39], v7 offset:8208\n\ts_waitcnt vmcnt(21)\n\tds_read_b128 a[240:243], v46 offset:24576\n\tds_read_b128 a[248:251], v46 offset:26624\n\tds_read_b128 a[244:247], v46 offset:25600\n\tds_read_b128 a[252:255], v46 offset:27648\n\ts_waitcnt lgkmcnt(4)\n\ts_waitcnt lgkmcnt(2)\n\tv_mfma_f32_16x16x32_bf16 v[4:7], a[240:243], v[8:11], 0\n\tv_mfma_f32_16x16x32_bf16 v[40:43], a[248:251], v[8:11], 0\n\tds_read_b128 a[240:243], v46 offset:28672\n\tds_read_b128 a[248:251], v46 offset:30720\n\ts_waitcnt lgkmcnt(2)\n\tv_mfma_f32_16x16x32_bf16 v[4:7], a[244:247], v[12:15], v[4:7]\n\tv_mfma_f32_16x16x32_bf16 v[40:43], a[252:255], v[12:15], v[40:43]\n\tds_read_b128 a[244:247], v46 offset:29696\n\tds_read_b128 a[252:255], v46 offset:31744\n\ts_waitcnt lgkmcnt(2)\n\tv_mfma_f32_16x16x32_bf16 v[4:7], a[240:243], v[16:19], v[4:7]\n\tv_mfma_f32_16x16x32_bf16 v[40:43], a[248:251], v[16:19], v[40:43]\n\tds_read_b128 a[240:243], v46 offset:32768\n\tds_read_b128 a[248:251], v46 offset:34816\n\ts_waitcnt lgkmcnt(2)\n\tv_mfma_f32_16x16x32_bf16 v[4:7], a[244:247], v[20:23], v[4:7]\n\tv_mfma_f32_16x16x32_bf16 v[40:43], a[252:255], v[20:23], v[40:43]\n\tds_read_b128 a[244:247], v46 offset:33792\n\tds_read_b128 a[252:255], v46 offset:35840\n\ts_waitcnt lgkmcnt(2)\n\tv_mfma_f32_16x16x32_bf16 v[4:7], a[240:243], v[24:27], v[4:7]\n\tv_mfma_f32_16x16x32_bf16 v[40:43], a[248:251], v[24:27], v[40:43]\n\tds_read_b128 a[240:243], v46 offset:0\n\tds_read_b128 a[248:251], v46 offset:2048\n\ts_waitcnt lgkmcnt(2)\n\tv_mfma_f32_16x16x32_bf16 v[4:7], a[244:247], v[28:31], v[4:7]\n\tv_mfma_f32_16x16x32_bf16 v[40:43], a[252:255], v[28:31], v[40:43]\n\tds_read_b128 a[244:247], v46 offset:1024\n\tds_read_b128 a[252:255], v46 offset:3072\n\ts_waitcnt lgkmcnt(2)\n\tv_mfma_f32_16x16x32_bf16 v[4:7], a[240:243], v[32:35], v[4:7]\n\tv_mfma_f32_16x16x32_bf16 v[40:43], a[248:251], v[32:35], v[40:43]\n\ts_waitcnt lgkmcnt(0)\n\tv_mfma_f32_16x16x32_bf16 v[4:7], a[244:247], v[36:39], v[4:7]\n\tv_mfma_f32_16x16x32_bf16 v[40:43], a[252:255], v[36:39], v[40:43]\n\ts_nop 7\n\ts_nop 0\n\tv_add_f32_dpp v8, v5, v4 row_ror:8 row_mask:0xf bank_mask:0x3\n\tv_add_f32_dpp v9, v7, v6 row_ror:8 row_mask:0xf bank_mask:0x3\n\tv_add_f32_dpp v10, v41, v40 row_ror:8 row_mask:0xf bank_mask:0x3\n\tv_add_f32_dpp v11, v43, v42 row_ror:8 row_mask:0xf bank_mask:0x3\n\tv_add_f32_dpp v8, v4, v5 row_ror:8 row_mask:0xf bank_mask:0xc\n\tv_add_f32_dpp v9, v6, v7 row_ror:8 row_mask:0xf bank_mask:0xc\n\tv_add_f32_dpp v10, v40, v41 row_ror:8 row_mask:0xf bank_mask:0xc\n\tv_add_f32_dpp v11, v42, v43 row_ror:8 row_mask:0xf bank_mask:0xc\n\tv_max3_f32 v12, v8, v9, v10\n\tv_max3_f32 v12, v12, v11, v56\n\ts_nop 1\n\tv_max_f32_dpp v12, v12, v12 quad_perm:[1,0,3,2] row_mask:0xf bank_mask:0xf\n\ts_nop 1\n\tv_max_f32_dpp v12, v12, v12 quad_perm:[2,3,0,1] row_mask:0xf bank_mask:0xf\n\tv_mov_b32 v13, v12\n\ts_nop 1\n\tv_permlane16_swap_b32 v12, v13\n\ts_nop 0\n\tv_max_f32 v12, v12, v13\n\tv_mov_b32 v13, v12\n\ts_nop 1\n\tv_permlane32_swap_b32 v12, v13\n\ts_nop 0\n\tv_max_f32 v12, v12, v13\n\tv_mul_f32 v15, %0, v12\n\tv_fma_f32 v13, v56, %0, -v15\n\tv_fma_f32 v8, v8, %0, -v15\n\tv_fma_f32 v9, v9, %0, -v15\n\tv_fma_f32 v10, v10, %0, -v15\n\tv_fma_f32 v11, v11, %0, -v15\n\tv_exp_f32 v13, v13\n\tv_exp_f32 v8, v8\n\tv_exp_f32 v9, v9\n\tv_exp_f32 v10, v10\n\tv_exp_f32 v11, v11\n\tv_mov_b32 v56, v12\n\tv_cvt_pk_bf16_f32 v4, v8, v8\n\tv_cvt_pk_bf16_f32 v5, v9, v9\n\tv_cvt_pk_bf16_f32 v6, v10, v10\n\tv_cvt_pk_bf16_f32 v7, v11, v11\n\tv_add_f32 v8, v8, v9\n\tv_add_f32 v10, v10, v11\n\tv_fma_f32 v57, v57, v13, v8\n\tv_mul_f32 v48, v48, v13\n\tv_mul_f32 v49, v49, v13\n\tv_mul_f32 v50, v50, v13\n\tv_mul_f32 v51, v51, v13\n\tv_add_f32 v57, v57, v10\n\tv_and_b32_dpp v16, v4, v60 quad_perm:[0,0,0,0] row_mask:0xf bank_mask:0xf\n\tv_and_b32_dpp v17, v4, v61 quad_perm:[0,0,0,0] row_mask:0xf bank_mask:0xf\n\tv_and_b32_dpp v18, v4, v62 quad_perm:[0,0,0,0] row_mask:0xf bank_mask:0xf\n\tv_and_b32_dpp v19, v4, v63 quad_perm:[0,0,0,0] row_mask:0xf bank_mask:0xf\n\tv_and_b32_dpp v20, v5, v60 quad_perm:[0,0,0,0] row_mask:0xf bank_mask:0xf\n\tv_and_b32_dpp v21, v5, v61 quad_perm:[0,0,0,0] row_mask:0xf bank_mask:0xf\n\tv_and_b32_dpp v22, v5, v62 quad_perm:[0,0,0,0] row_mask:0xf bank_mask:0xf\n\tv_and_b32_dpp v23, v5, v63 quad_perm:[0,0,0,0] row_mask:0xf bank_mask:0xf\n\t1:\n\ts_memrealtime s[98:99]\n\ts_waitcnt lgkmcnt(0)\n\ts_sub_u32 s98, s98, %1\n\ts_mul_i32 s99, %2, 13\n\ts_lshl_b32 s98, s98, 4\n\ts_cmp_lt_u32 s98, s99\n\ts_cbranch_scc1 1b\n\tv_xor_b32 v14, 0x4200000, v44\n\ts_add_u32 m0, %4, 4096\n\ts_nop 0\n\tglobal_load_lds_dwordx4 v14, %3 offset:0 nt\n\ts_add_u32 m0, %4, 4992\n\ts_nop 0\n\tglobal_load_lds_dwordx4 v14, %3 offset:128 nt\n\ts_add_u32 m0, %4, 5632\n\ts_nop 0\n\tglobal_load_lds_dwordx4 v14, %3 offset:512 nt\n\ts_add_u32 m0, %4, 6528\n\ts_nop 0\n\tglobal_load_lds_dwordx4 v14, %3 offset:640 nt\n\ts_waitcnt vmcnt(16)\n\tv_mfma_f32_16x16x32_bf16 v[48:51], v[192:195], v[16:19], v[48:51]\n\tv_and_b32_dpp v16, v6, v60 quad_perm:[0,0,0,0] row_mask:0xf bank_mask:0xf\n\tv_and_b32_dpp v17, v6, v61 quad_perm:[0,0,0,0] row_mask:0xf bank_mask:0xf\n\tv_and_b32_dpp v18, v6, v62 quad_perm:[0,0,0,0] row_mask:0xf bank_mask:0xf\n\tv_and_b32_dpp v19, v6, v63 quad_perm:[0,0,0,0] row_mask:0xf bank_mask:0xf\n\tv_mfma_f32_16x16x32_bf16 v[48:51], v[196:199], v[20:23], v[48:51]\n\tv_and_b32_dpp v20, v7, v60 quad_perm:[0,0,0,0] row_mask:0xf bank_mask:0xf\n\tv_and_b32_dpp v21, v7, v61 quad_perm:[0,0,0,0] row_mask:0xf bank_mask:0xf\n\tv_and_b32_dpp v22, v7, v62 quad_perm:[0,0,0,0] row_mask:0xf bank_mask:0xf\n\tv_and_b32_dpp v23, v7, v63 quad_perm:[0,0,0,0] row_mask:0xf bank_mask:0xf\n\tv_mfma_f32_16x16x32_bf16 v[48:51], v[200:203], v[16:19], v[48:51]\n\tv_and_b32_dpp v16, v4, v60 quad_perm:[1,1,1,1] row_mask:0xf bank_mask:0xf\n\tv_and_b32_dpp v17, v4, v61 quad_perm:[1,1,1,1] row_mask:0xf bank_mask:0xf\n\tv_and_b32_dpp v18, v4, v62 quad_perm:[1,1,1,1] row_mask:0xf bank_mask:0xf\n\tv_and_b32_dpp v19, v4, v63 quad_perm:[1,1,1,1] row_mask:0xf bank_mask:0xf\n\tv_mfma_f32_16x16x32_bf16 v[48:51], v[204:207], v[20:23], v[48:51]\n\tv_and_b32_dpp v20, v5, v60 quad_perm:[1,1,1,1] row_mask:0xf bank_mask:0xf\n\tv_and_b32_dpp v21, v5, v61 quad_perm:[1,1,1,1] row_mask:0xf bank_mask:0xf\n\tv_and_b32_dpp v22, v5, v62 quad_perm:[1,1,1,1] row_mask:0xf bank_mask:0xf\n\tv_and_b32_dpp v23, v5, v63 quad_perm:[1,1,1,1] row_mask:0xf bank_mask:0xf\n\t1:\n\ts_memrealtime s[98:99]\n\ts_waitcnt lgkmcnt(0)\n\ts_sub_u32 s98, s98, %1\n\ts_mul_i32 s99, %2, 14\n\ts_lshl_b32 s98, s98, 4\n\ts_cmp_lt_u32 s98, s99\n\ts_cbranch_scc1 1b\n\tv_xor_b32 v14, 0x4240000, v44\n\ts_add_u32 m0, %4, 8192\n\ts_nop 0\n\tglobal_load_lds_dwordx4 v14, %3 offset:0 nt\n\ts_add_u32 m0, %4, 9088\n\ts_nop 0\n\tglobal_load_lds_dwordx4 v14, %3 offset:128 nt\n\ts_add_u32 m0, %4, 9728\n\ts_nop 0\n\tglobal_load_lds_dwordx4 v14, %3 offset:512 nt\n\ts_add_u32 m0, %4, 10624\n\ts_nop 0\n\tglobal_load_lds_dwordx4 v14, %3 offset:640 nt\n\ts_waitcnt vmcnt(16)\n\tv_mfma_f32_16x16x32_bf16 v[48:51], v[208:211], v[16:19], v[48:51]\n\tv_and_b32_dpp v16, v6, v60 quad_perm:[1,1,1,1] row_mask:0xf bank_mask:0xf\n\tv_and_b32_dpp v17, v6, v61 quad_perm:[1,1,1,1] row_mask:0xf bank_mask:0xf\n\tv_and_b32_dpp v18, v6, v62 quad_perm:[1,1,1,1] row_mask:0xf bank_mask:0xf\n\tv_and_b32_dpp v19, v6, v63 quad_perm:[1,1,1,1] row_mask:0xf bank_mask:0xf\n\tv_mfma_f32_16x16x32_bf16 v[48:51], v[212:215], v[20:23], v[48:51]\n\tv_and_b32_dpp v20, v7, v60 quad_perm:[1,1,1,1] row_mask:0xf bank_mask:0xf\n\tv_and_b32_dpp v21, v7, v61 quad_perm:[1,1,1,1] row_mask:0xf bank_mask:0xf\n\tv_and_b32_dpp v22, v7, v62 quad_perm:[1,1,1,1] row_mask:0xf bank_mask:0xf\n\tv_and_b32_dpp v23, v7, v63 quad_perm:[1,1,1,1] row_mask:0xf bank_mask:0xf\n\tv_mfma_f32_16x16x32_bf16 v[48:51], v[216:219], v[16:19], v[48:51]\n\tv_and_b32_dpp v16, v4, v60 quad_perm:[2,2,2,2] row_mask:0xf bank_mask:0xf\n\tv_and_b32_dpp v17, v4, v61 quad_perm:[2,2,2,2] row_mask:0xf bank_mask:0xf\n\tv_and_b32_dpp v18, v4, v62 quad_perm:[2,2,2,2] row_mask:0xf bank_mask:0xf\n\tv_and_b32_dpp v19, v4, v63 quad_perm:[2,2,2,2] row_mask:0xf bank_mask:0xf\n\tv_mfma_f32_16x16x32_bf16 v[48:51], v[220:223], v[20:23], v[48:51]\n\tv_and_b32_dpp v20, v5, v60 quad_perm:[2,2,2,2] row_mask:0xf bank_mask:0xf\n\tv_and_b32_dpp v21, v5, v61 quad_perm:[2,2,2,2] row_mask:0xf bank_mask:0xf\n\tv_and_b32_dpp v22, v5, v62 quad_perm:[2,2,2,2] row_mask:0xf bank_mask:0xf\n\tv_and_b32_dpp v23, v5, v63 quad_perm:[2,2,2,2] row_mask:0xf bank_mask:0xf\n\t1:\n\ts_memrealtime s[98:99]\n\ts_waitcnt lgkmcnt(0)\n\ts_sub_u32 s98, s98, %1\n\ts_mul_i32 s99, %2, 15\n\ts_lshl_b32 s98, s98, 4\n\ts_cmp_lt_u32 s98, s99\n\ts_cbranch_scc1 1b\n\tv_xor_b32 v14, 0x4280000, v44\n\tglobal_load_dwordx4 v[128:131], v14, %3 offset:0 nt\n\tglobal_load_dwordx4 v[132:135], v14, %3 offset:128 nt\n\tglobal_load_dwordx4 v[136:139], v14, %3 offset:512 nt\n\tglobal_load_dwordx4 v[140:143], v14, %3 offset:640 nt\n\ts_waitcnt vmcnt(16)\n\tv_mfma_f32_16x16x32_bf16 v[48:51], v[224:227], v[16:19], v[48:51]\n\tv_and_b32_dpp v16, v6, v60 quad_perm:[2,2,2,2] row_mask:0xf bank_mask:0xf\n\tv_and_b32_dpp v17, v6, v61 quad_perm:[2,2,2,2] row_mask:0xf bank_mask:0xf\n\tv_and_b32_dpp v18, v6, v62 quad_perm:[2,2,2,2] row_mask:0xf bank_mask:0xf\n\tv_and_b32_dpp v19, v6, v63 quad_perm:[2,2,2,2] row_mask:0xf bank_mask:0xf\n\tv_mfma_f32_16x16x32_bf16 v[48:51], v[228:231], v[20:23], v[48:51]\n\tv_and_b32_dpp v20, v7, v60 quad_perm:[2,2,2,2] row_mask:0xf bank_mask:0xf\n\tv_and_b32_dpp v21, v7, v61 quad_perm:[2,2,2,2] row_mask:0xf bank_mask:0xf\n\tv_and_b32_dpp v22, v7, v62 quad_perm:[2,2,2,2] row_mask:0xf bank_mask:0xf\n\tv_and_b32_dpp v23, v7, v63 quad_perm:[2,2,2,2] row_mask:0xf bank_mask:0xf\n\tv_mfma_f32_16x16x32_bf16 v[48:51], v[232:235], v[16:19], v[48:51]\n\tv_and_b32_dpp v16, v4, v60 quad_perm:[3,3,3,3] row_mask:0xf bank_mask:0xf\n\tv_and_b32_dpp v17, v4, v61 quad_perm:[3,3,3,3] row_mask:0xf bank_mask:0xf\n\tv_and_b32_dpp v18, v4, v62 quad_perm:[3,3,3,3] row_mask:0xf bank_mask:0xf\n\tv_and_b32_dpp v19, v4, v63 quad_perm:[3,3,3,3] row_mask:0xf bank_mask:0xf\n\tv_mfma_f32_16x16x32_bf16 v[48:51], v[236:239], v[20:23], v[48:51]\n\tv_and_b32_dpp v20, v5, v60 quad_perm:[3,3,3,3] row_mask:0xf bank_mask:0xf\n\tv_and_b32_dpp v21, v5, v61 quad_perm:[3,3,3,3] row_mask:0xf bank_mask:0xf\n\tv_and_b32_dpp v22, v5, v62 quad_perm:[3,3,3,3] row_mask:0xf bank_mask:0xf\n\tv_and_b32_dpp v23, v5, v63 quad_perm:[3,3,3,3] row_mask:0xf bank_mask:0xf\n\t1:\n\ts_memrealtime s[98:99]\n\ts_waitcnt lgkmcnt(0)\n\ts_sub_u32 s98, s98, %1\n\ts_mul_i32 s99, %2, 16\n\ts_lshl_b32 s98, s98, 4\n\ts_cmp_lt_u32 s98, s99\n\ts_cbranch_scc1 1b\n\tv_xor_b32 v14, 0x42c0000, v44\n\tglobal_load_dwordx4 v[144:147], v14, %3 offset:0 nt\n\tglobal_load_dwordx4 v[148:151], v14, %3 offset:128 nt\n\tglobal_load_dwordx4 v[152:155], v14, %3 offset:512 nt\n\tglobal_load_dwordx4 v[156:159], v14, %3 offset:640 nt\n\ts_waitcnt vmcnt(16)\n\tv_mfma_f32_16x16x32_bf16 v[48:51], v[64:67], v[16:19], v[48:51]\n\tv_and_b32_dpp v16, v6, v60 quad_perm:[3,3,3,3] row_mask:0xf bank_mask:0xf\n\tv_and_b32_dpp v17, v6, v61 quad_perm:[3,3,3,3] row_mask:0xf bank_mask:0xf\n\tv_and_b32_dpp v18, v6, v62 quad_perm:[3,3,3,3] row_mask:0xf bank_mask:0xf\n\tv_and_b32_dpp v19, v6, v63 quad_perm:[3,3,3,3] row_mask:0xf bank_mask:0xf\n\tv_mfma_f32_16x16x32_bf16 v[48:51], v[68:71], v[20:23], v[48:51]\n\tv_and_b32_dpp v20, v7, v60 quad_perm:[3,3,3,3] row_mask:0xf bank_mask:0xf\n\tv_and_b32_dpp v21, v7, v61 quad_perm:[3,3,3,3] row_mask:0xf bank_mask:0xf\n\tv_and_b32_dpp v22, v7, v62 quad_perm:[3,3,3,3] row_mask:0xf bank_mask:0xf\n\tv_and_b32_dpp v23, v7, v63 quad_perm:[3,3,3,3] row_mask:0xf bank_mask:0xf\n\tv_mfma_f32_16x16x32_bf16 v[48:51], v[72:75], v[16:19], v[48:51]\n\ts_nop 0\n\tv_mfma_f32_16x16x32_bf16 v[48:51], v[76:79], v[20:23], v[48:51]" :  : "s"(CSC), "s"(tb), "s"(d16), "s"(Kb), "s"(lb) : "memory","scc","vcc","s98","s99","v4","v5","v6","v7","v8","v9","v10","v11","v12","v13","v14","v15","v16","v17","v18","v19","v20","v21","v22","v23","v24","v25","v26","v27","v28","v29","v30","v31","v32","v33","v34","v35","v36","v37","v38","v39","v40","v41","v42","v43");
  pace(tb, (d16 * 17u) >> 4);
  asm volatile("v_xor_b32 v43, 0x4200000, v45\n\tglobal_load_dwordx4 v[160:163], v43, %0 offset:0 nt\n\tglobal_load_dwordx4 v[164:167], v43, %0 offset:256 nt\n\tglobal_load_dwordx4 v[168:171], v43, %0 offset:512 nt\n\tglobal_load_dwordx4 v[172:175], v43, %0 offset:768 nt" :  : "s"(Vb) : "memory","scc","v43","v42","s98","s99");
  pace(tb, (d16 * 18u) >> 4);
  asm volatile("v_xor_b32 v43, 0x4240000, v45\n\tglobal_load_dwordx4 v[176:179], v43, %0 offset:0 nt\n\tglobal_load_dwordx4 v[180:183], v43, %0 offset:256 nt\n\tglobal_load_dwordx4 v[184:187], v43, %0 offset:512 nt\n\tglobal_load_dwordx4 v[188:191], v43, %0 offset:768 nt" :  : "s"(Vb) : "memory","scc","v43","v42","s98","s99");
  pace(tb, (d16 * 19u) >> 4);
  asm volatile("v_xor_b32 v43, 0x4280000, v45\n\tglobal_load_dwordx4 a[0:3], v43, %0 offset:0 nt\n\tglobal_load_dwordx4 a[4:7], v43, %0 offset:256 nt\n\tglobal_load_dwordx4 a[8:11], v43, %0 offset:512 nt\n\tglobal_load_dwordx4 a[12:15], v43, %0 offset:768 nt" :  : "s"(Vb) : "memory","scc","v43","v42","s98","s99");
  pace(tb, (d16 * 20u) >> 4);
  asm volatile("v_xor_b32 v43, 0x42c0000, v45\n\tglobal_load_dwordx4 a[16:19], v43, %0 offset:0 nt\n\tglobal_load_dwordx4 a[20:23], v43, %0 offset:256 nt\n\tglobal_load_dwordx4 a[24:27], v43, %0 offset:512 nt\n\tglobal_load_dwordx4 a[28:31], v43, %0 offset:768 nt" :  : "s"(Vb) : "memory","scc","v43","v42","s98","s99");
  asm volatile("v_mov_b32 v40, 7776\n\ts_mov_b32 vcc_lo, 0x11111111\n\ts_mov_b32 vcc_hi, 0x11111111\n\tv_cndmask_b32 v4, v40, v47, vcc\n\ts_lshl_b64 vcc, vcc, 1\n\tv_cndmask_b32 v5, v40, v47, vcc\n\ts_lshl_b64 vcc, vcc, 1\n\tv_cndmask_b32 v6, v40, v47, vcc\n\ts_lshl_b64 vcc, vcc, 1\n\tv_cndmask_b32 v7, v40, v47, vcc\n\tds_read_b128 v[24:27], v6 offset:8192\n\tds_read_b128 v[28:31], v6 offset:8208\n\tds_read_b128 v[32:35], v7 offset:8192\n\tds_read_b128 v[36:39], v7 offset:8208\n\tds_read_b128 v[8:11], v4 offset:8192\n\tds_read_b128 v[12:15], v4 offset:8208\n\tds_read_b128 v[16:19], v5 offset:8192\n\tds_read_b128 v[20:23], v5 offset:8208\n\ts_waitcnt vmcnt(16)\n\tds_read_b128 a[240:243], v46 offset:4096\n\tds_read_b128 a[248:251], v46 offset:6144\n\tds_read_b128 a[244:247], v46 offset:5120\n\tds_read_b128 a[252:255], v46 offset:7168\n\ts_waitcnt lgkmcnt(4)\n\tv_mfma_f32_16x16x32_bf16 v[4:7], v[128:131], v[24:27], 0\n\tv_mfma_f32_16x16x32_bf16 v[40:43], v[136:139], v[24:27], 0\n\tv_mfma_f32_16x16x32_bf16 v[4:7], v[132:135], v[28:31], v[4:7]\n\tv_mfma_f32_16x16x32_bf16 v[40:43], v[140:143], v[28:31], v[40:43]\n\tv_mfma_f32_16x16x32_bf16 v[4:7], v[144:147], v[32:35], v[4:7]\n\tv_mfma_f32_16x16x32_bf16 v[40:43], v[152:155], v[32:35], v[40:43]\n\tv_mfma_f32_16x16x32_bf16 v[4:7], v[148:151], v[36:39], v[4:7]\n\tv_mfma_f32_16x16x32_bf16 v[40:43], v[156:159], v[36:39], v[40:43]\n\ts_waitcnt lgkmcnt(2)\n\tv_mfma_f32_16x16x32_bf16 v[4:7], a[240:243], v[8:11], v[4:7]\n\tv_mfma_f32_16x16x32_bf16 v[40:43], a[248:251], v[8:11], v[40:43]\n\tds_read_b128 a[240:243], v46 offset:8192\n\tds_read_b128 a[248:251], v46 offset:10240\n\ts_waitcnt lgkmcnt(2)\n\tv_mfma_f32_16x16x32_bf16 v[4:7], a[244:247], v[12:15], v[4:7]\n\tv_mfma_f32_16x16x32_bf16 v[40:43], a[252:255], v[12:15], v[40:43]\n\tds_read_b128 a[244:247], v46 offset:9216\n\tds_read_b128 a[252:255], v46 offset:11264\n\ts_waitcnt lgkmcnt(2)\n\tv_mfma_f32_16x16x32_bf16 v[4:7], a[240:243], v[16:19], v[4:7]\n\tv_mfma_f32_16x16x32_bf16 v[40:43], a[248:251], v[16:19], v[40:43]\n\ts_waitcnt lgkmcnt(0)\n\tv_mfma_f32_16x16x32_bf16 v[4:7], a[244:247], v[20:23], v[4:7]\n\tv_mfma_f32_16x16x32_bf16 v[40:43], a[252:255], v[20:23], v[40:43]\n\ts_nop 7\n\ts_nop 0\n\tv_add_f32_dpp v8, v5, v4 row_ror:8 row_mask:0xf bank_mask:0x3\n\tv_add_f32_dpp v9, v7, v6 row_ror:8 row_mask:0xf bank_mask:0x3\n\tv_add_f32_dpp v10, v41, v40 row_ror:8 row_mask:0xf bank_mask:0x3\n\tv_add_f32_dpp v11, v43, v42 row_ror:8 row_mask:0xf bank_mask:0x3\n\tv_add_f32_dpp v8, v4, v5 row_ror:8 row_mask:0xf bank_mask:0xc\n\tv_add_f32_dpp v9, v6, v7 row_ror:8 row_mask:0xf bank_mask:0xc\n\tv_add_f32_dpp v10, v40, v41 row_ror:8 row_mask:0xf bank_mask:0xc\n\tv_add_f32_dpp v11, v42, v43 row_ror:8 row_mask:0xf bank_mask:0xc\n\tv_max3_f32 v12, v8, v9, v10\n\tv_max3_f32 v12, v12, v11, v56\n\ts_nop 1\n\tv_max_f32_dpp v12, v12, v12 quad_perm:[1,0,3,2] row_mask:0xf bank_mask:0xf\n\ts_nop 1\n\tv_max_f32_dpp v12, v12, v12 quad_perm:[2,3,0,1] row_mask:0xf bank_mask:0xf\n\tv_mov_b32 v13, v12\n\ts_nop 1\n\tv_permlane16_swap_b32 v12, v13\n\ts_nop 0\n\tv_max_f32 v12, v12, v13\n\tv_mov_b32 v13, v12\n\ts_nop 1\n\tv_permlane32_swap_b32 v12, v13\n\ts_nop 0\n\tv_max_f32 v12, v12, v13\n\tv_mul_f32 v15, %0, v12\n\tv_fma_f32 v13, v56, %0, -v15\n\tv_fma_f32 v8, v8, %0, -v15\n\tv_fma_f32 v9, v9, %0, -v15\n\tv_fma_f32 v10, v10, %0, -v15\n\tv_fma_f32 v11, v11, %0, -v15\n\tv_exp_f32 v13, v13\n\tv_exp_f32 v8, v8\n\tv_exp_f32 v9, v9\n\tv_exp_f32 v10, v10\n\tv_exp_f32 v11, v11\n\tv_mov_b32 v56, v12\n\tv_cvt_pk_bf16_f32 v4, v8, v8\n\tv_cvt_pk_bf16_f32 v5, v9, v9\n\tv_cvt_pk_bf16_f32 v6, v10, v10\n\tv_cvt_pk_bf16_f32 v7, v11, v11\n\tv_add_f32 v8, v8, v9\n\tv_add_f32 v10, v10, v11\n\tv_fma_f32 v57, v57, v13, v8\n\tv_mul_f32 v48, v48, v13\n\tv_mul_f32 v49, v49, v13\n\tv_mul_f32 v50, v50, v13\n\tv_mul_f32 v51, v51, v13\n\tv_add_f32 v57, v57, v10\n\tv_and_b32_dpp v16, v4, v60 quad_perm:[0,0,0,0] row_mask:0xf bank_mask:0xf\n\tv_and_b32_dpp v17, v4, v61 quad_perm:[0,0,0,0] row_mask:0xf bank_mask:0xf\n\tv_and_b32_dpp v18, v4, v62 quad_perm:[0,0,0,0] row_mask:0xf bank_mask:0xf\n\tv_and_b32_dpp v19, v4, v63 quad_perm:[0,0,0,0] row_mask:0xf bank_mask:0xf\n\tv_and_b32_dpp v20, v5, v60 quad_perm:[0,0,0,0] row_mask:0xf bank_mask:0xf\n\tv_and_b32_dpp v21, v5, v61 quad_perm:[0,0,0,0] row_mask:0xf bank_mask:0xf\n\tv_and_b32_dpp v22, v5, v62 quad_perm:[0,0,0,0] row_mask:0xf bank_mask:0xf\n\tv_and_b32_dpp v23, v5, v63 quad_perm:[0,0,0,0] row_mask:0xf bank_mask:0xf\n\t1:\n\ts_memrealtime s[98:99]\n\ts_waitcnt lgkmcnt(0)\n\ts_sub_u32 s98, s98, %1\n\ts_mul_i32 s99, %2, 21\n\ts_lshl_b32 s98, s98, 4\n\ts_cmp_lt_u32 s98, s99\n\ts_cbranch_scc1 1b\n\tv_xor_b32 v14, 0x4300000, v44\n\tglobal_load_dwordx4 a[32:35], v14, %3 offset:0 nt\n\tglobal_load_dwordx4 a[36:39], v14, %3 offset:128 nt\n\tglobal_load_dwordx4 a[40:43], v14, %3 offset:512 nt\n\tglobal_load_dwordx4 a[44:47], v14, %3 offset:640 nt\n\ts_waitcnt vmcnt(16)\n\tv_mfma_f32_16x16x32_bf16 v[48:51], v[160:163], v[16:19], v[48:51]\n\tv_and_b32_dpp v16, v6, v60 quad_perm:[0,0,0,0] row_mask:0xf bank_mask:0xf\n\tv_and_b32_dpp v17, v6, v61 quad_perm:[0,0,0,0] row_mask:0xf bank_mask:0xf\n\tv_and_b32_dpp v18, v6, v62 quad_perm:[0,0,0,0] row_mask:0xf bank_mask:0xf\n\tv_and_b32_dpp v19, v6, v63 quad_perm:[0,0,0,0] row_mask:0xf bank_mask:0xf\n\tv_mfma_f32_16x16x32_bf16 v[48:51], v[164:167], v[20:23], v[48:51]\n\tv_and_b32_dpp v20, v7, v60 quad_perm:[0,0,0,0] row_mask:0xf bank_mask:0xf\n\tv_and_b32_dpp v21, v7, v61 quad_perm:[0,0,0,0] row_mask:0xf bank_mask:0xf\n\tv_and_b32_dpp v22, v7, v62 quad_perm:[0,0,0,0] row_mask:0xf bank_mask:0xf\n\tv_and_b32_dpp v23, v7, v63 quad_perm:[0,0,0,0] row_mask:0xf bank_mask:0xf\n\tv_mfma_f32_16x16x32_bf16 v[48:51], v[168:171], v[16:19], v[48:51]\n\tv_and_b32_dpp v16, v4, v60 quad_perm:[1,1,1,1] row_mask:0xf bank_mask:0xf\n\tv_and_b32_dpp v17, v4, v61 quad_perm:[1,1,1,1] row_mask:0xf bank_mask:0xf\n\tv_and_b32_dpp v18, v4, v62 quad_perm:[1,1,1,1] row_mask:0xf bank_mask:0xf\n\tv_and_b32_dpp v19, v4, v63 quad_perm:[1,1,1,1] row_mask:0xf bank_mask:0xf\n\tv_mfma_f32_16x16x32_bf16 v[48:51], v[172:175], v[20:23], v[48:51]\n\tv_and_b32_dpp v20, v5, v60 quad_perm:[1,1,1,1] row_mask:0xf bank_mask:0xf\n\tv_and_b32_dpp v21, v5, v61 quad_perm:[1,1,1,1] row_mask:0xf bank_mask:0xf\n\tv_and_b32_dpp v22, v5, v62 quad_perm:[1,1,1,1] row_mask:0xf bank_mask:0xf\n\tv_and_b32_dpp v23, v5, v63 quad_perm:[1,1,1,1] row_mask:0xf bank_mask:0xf\n\t1:\n\ts_memrealtime s[98:99]\n\ts_waitcnt lgkmcnt(0)\n\ts_sub_u32 s98, s98, %1\n\ts_mul_i32 s99, %2, 22\n\ts_lshl_b32 s98, s98, 4\n\ts_cmp_lt_u32 s98, s99\n\ts_cbranch_scc1 1b\n\tv_xor_b32 v14, 0x4340000, v44\n\tglobal_load_dwordx4 a[48:51], v14, %3 offset:0 nt\n\tglobal_load_dwordx4 a[52:55], v14, %3 offset:128 nt\n\tglobal_load_dwordx4 a[56:59], v14, %3 offset:512 nt\n\tglobal_load_dwordx4 a[60:63], v14, %3 offset:640 nt\n\ts_waitcnt vmcnt(16)\n\tv_mfma_f32_16x16x32_bf16 v[48:51], v[176:179], v[16:19], v[48:51]\n\tv_and_b32_dpp v16, v6, v60 quad_perm:[1,1,1,1] row_mask:0xf bank_mask:0xf\n\tv_and_b32_dpp v17, v6, v61 quad_perm:[1,1,1,1] row_mask:0xf bank_mask:0xf\n\tv_and_b32_dpp v18, v6, v62 quad_perm:[1,1,1,1] row_mask:0xf bank_mask:0xf\n\tv_and_b32_dpp v19, v6, v63 quad_perm:[1,1,1,1] row_mask:0xf bank_mask:0xf\n\tv_mfma_f32_16x16x32_bf16 v[48:51], v[180:183], v[20:23], v[48:51]\n\tv_and_b32_dpp v20, v7, v60 quad_perm:[1,1,1,1] row_mask:0xf bank_mask:0xf\n\tv_and_b32_dpp v21, v7, v61 quad_perm:[1,1,1,1] row_mask:0xf bank_mask:0xf\n\tv_and_b32_dpp v22, v7, v62 quad_perm:[1,1,1,1] row_mask:0xf bank_mask:0xf\n\tv_and_b32_dpp v23, v7, v63 quad_perm:[1,1,1,1] row_mask:0xf bank_mask:0xf\n\tv_mfma_f32_16x16x32_bf16 v[48:51], v[184:187], v[16:19], v[48:51]\n\tv_and_b32_dpp v16, v4, v60 quad_perm:[2,2,2,2] row_mask:0xf bank_mask:0xf\n\tv_and_b32_dpp v17, v4, v61 quad_perm:[2,2,2,2] row_mask:0xf bank_mask:0xf\n\tv_and_b32_dpp v18, v4, v62 quad_perm:[2,2,2,2] row_mask:0xf bank_mask:0xf\n\tv_and_b32_dpp v19, v4, v63 quad_perm:[2,2,2,2] row_mask:0xf bank_mask:0xf\n\tv_mfma_f32_16x16x32_bf16 v[48:51], v[188:191], v[20:23], v[48:51]\n\tv_and_b32_dpp v20, v5, v60 quad_perm:[2,2,2,2] row_mask:0xf bank_mask:0xf\n\tv_and_b32_dpp v21, v5, v61 quad_perm:[2,2,2,2] row_mask:0xf bank_mask:0xf\n\tv_and_b32_dpp v22, v5, v62 quad_perm:[2,2,2,2] row_mask:0xf bank_mask:0xf\n\tv_and_b32_dpp v23, v5, v63 quad_perm:[2,2,2,2] row_mask:0xf bank_mask:0xf\n\t1:\n\ts_memrealtime s[98:99]\n\ts_waitcnt lgkmcnt(0)\n\ts_sub_u32 s98, s98, %1\n\ts_mul_i32 s99, %2, 23\n\ts_lshl_b32 s98, s98, 4\n\ts_cmp_lt_u32 s98, s99\n\ts_cbranch_scc1 1b\n\tv_xor_b32 v14, 0x4380000, v44\n\tglobal_load_dwordx4 a[176:179], v14, %3 offset:0 nt\n\tglobal_load_dwordx4 a[180:183], v14, %3 offset:128 nt\n\tglobal_load_dwordx4 a[184:187], v14, %3 offset:512 nt\n\tglobal_load_dwordx4 a[188:191], v14, %3 offset:640 nt\n\ts_waitcnt vmcnt(16)\n\tv_mfma_f32_16x16x32_bf16 v[48:51], a[0:3], v[16:19], v[48:51]\n\tv_and_b32_dpp v16, v6, v60 quad_perm:[2,2,2,2] row_mask:0xf bank_mask:0xf\n\tv_and_b32_dpp v17, v6, v61 quad_perm:[2,2,2,2] row_mask:0xf bank_mask:0xf\n\tv_and_b32_dpp v18, v6, v62 quad_perm:[2,2,2,2] row_mask:0xf bank_mask:0xf\n\tv_and_b32_dpp v19, v6, v63 quad_perm:[2,2,2,2] row_mask:0xf bank_mask:0xf\n\tv_mfma_f32_16x16x32_bf16 v[48:51], a[4:7], v[20:23], v[48:51]\n\tv_and_b32_dpp v20, v7, v60 quad_perm:[2,2,2,2] row_mask:0xf bank_mask:0xf\n\tv_and_b32_dpp v21, v7, v61 quad_perm:[2,2,2,2] row_mask:0xf bank_mask:0xf\n\tv_and_b32_dpp v22, v7, v62 quad_perm:[2,2,2,2] row_mask:0xf bank_mask:0xf\n\tv_and_b32_dpp v23, v7, v63 quad_perm:[2,2,2,2] row_mask:0xf bank_mask:0xf\n\tv_mfma_f32_16x16x32_bf16 v[48:51], a[8:11], v[16:19], v[48:51]\n\tv_and_b32_dpp v16, v4, v60 quad_perm:[3,3,3,3] row_mask:0xf bank_mask:0xf\n\tv_and_b32_dpp v17, v4, v61 quad_perm:[3,3,3,3] row_mask:0xf bank_mask:0xf\n\tv_and_b32_dpp v18, v4, v62 quad_perm:[3,3,3,3] row_mask:0xf bank_mask:0xf\n\tv_and_b32_dpp v19, v4, v63 quad_perm:[3,3,3,3] row_mask:0xf bank_mask:0xf\n\tv_mfma_f32_16x16x32_bf16 v[48:51], a[12:15], v[20:23], v[48:51]\n\tv_and_b32_dpp v20, v5, v60 quad_perm:[3,3,3,3] row_mask:0xf bank_mask:0xf\n\tv_and_b32_dpp v21, v5, v61 quad_perm:[3,3,3,3] row_mask:0xf bank_mask:0xf\n\tv_and_b32_dpp v22, v5, v62 quad_perm:[3,3,3,3] row_mask:0xf bank_mask:0xf\n\tv_and_b32_dpp v23, v5, v63 quad_perm:[3,3,3,3] row_mask:0xf bank_mask:0xf\n\t1:\n\ts_memrealtime s[98:99]\n\ts_waitcnt lgkmcnt(0)\n\ts_sub_u32 s98, s98, %1\n\ts_mul_i32 s99, %2, 24\n\ts_lshl_b32 s98, s98, 4\n\ts_cmp_lt_u32 s98, s99\n\ts_cbranch_scc1 1b\n\tv_xor_b32 v14, 0x43c0000, v44\n\tglobal_load_dwordx4 a[64:67], v14, %3 offset:0 nt\n\tglobal_load_dwordx4 a[68:71], v14, %3 offset:128 nt\n\tglobal_load_dwordx4 a[72:75], v14, %3 offset:512 nt\n\tglobal_load_dwordx4 a[76:79], v14, %3 offset:640 nt\n\ts_waitcnt vmcnt(16)\n\tv_mfma_f32_16x16x32_bf16 v[48:51], a[16:19], v[16:19], v[48:51]\n\tv_and_b32_dpp v16, v6, v60 quad_perm:[3,3,3,3] row_mask:0xf bank_mask:0xf\n\tv_and_b32_dpp v17, v6, v61 quad_perm:[3,3,3,3] row_mask:0xf bank_mask:0xf\n\tv_and_b32_dpp v18, v6, v62 quad_perm:[3,3,3,3] row_mask:0xf bank_mask:0xf\n\tv_and_b32_dpp v19, v6, v63 quad_perm:[3,3,3,3] row_mask:0xf bank_mask:0xf\n\tv_mfma_f32_16x16x32_bf16 v[48:51], a[20:23], v[20:23], v[48:51]\n\tv_and_b32_dpp v20, v7, v60 quad_perm:[3,3,3,3] row_mask:0xf bank_mask:0xf\n\tv_and_b32_dpp v21, v7, v61 quad_perm:[3,3,3,3] row_mask:0xf bank_mask:0xf\n\tv_and_b32_dpp v22, v7, v62 quad_perm:[3,3,3,3] row_mask:0xf bank_mask:0xf\n\tv_and_b32_dpp v23, v7, v63 quad_perm:[3,3,3,3] row_mask:0xf bank_mask:0xf\n\tv_mfma_f32_16x16x32_bf16 v[48:51], a[24:27], v[16:19], v[48:51]\n\ts_nop 0\n\tv_mfma_f32_16x16x32_bf16 v[48:51], a[28:31], v[20:23], v[48:51]" :  : "s"(CSC), "s"(tb), "s"(d16), "s"(Kb) : "memory","scc","vcc","s98","s99","v4","v5","v6","v7","v8","v9","v10","v11","v12","v13","v14","v15","v16","v17","v18","v19","v20","v21","v22","v23","v24","v25","v26","v27","v28","v29","v30","v31","v32","v33","v34","v35","v36","v37","v38","v39","v40","v41","v42","v43");
  CODE_PREFETCH("a[192:195]");
  pace(tb, (d16 * 25u) >> 4);
  asm volatile("v_xor_b32 v43, 0x4300000, v45\n\tglobal_load_dwordx4 a[80:83], v43, %0 offset:0 nt\n\tglobal_load_dwordx4 a[84:87], v43, %0 offset:256 nt\n\tglobal_load_dwordx4 a[88:91], v43, %0 offset:512 nt\n\tglobal_load_dwordx4 a[92:95], v43, %0 offset:768 nt" :  : "s"(Vb) : "memory","scc","v43","v42","s98","s99");
  pace(tb, (d16 * 26u) >> 4);
  asm volatile("v_xor_b32 v43, 0x4340000, v45\n\tglobal_load_dwordx4 a[96:99], v43, %0 offset:0 nt\n\tglobal_load_dwordx4 a[100:103], v43, %0 offset:256 nt\n\tglobal_load_dwordx4 a[104:107], v43, %0 offset:512 nt\n\tglobal_load_dwordx4 a[108:111], v43, %0 offset:768 nt" :  : "s"(Vb) : "memory","scc","v43","v42","s98","s99");
  pace(tb, (d16 * 27u) >> 4);
  asm volatile("v_xor_b32 v43, 0x4380000, v45\n\tglobal_load_dwordx4 a[112:115], v43, %0 offset:0 nt\n\tglobal_load_dwordx4 a[116:119], v43, %0 offset:256 nt\n\tglobal_load_dwordx4 a[120:123], v43, %0 offset:512 nt\n\tglobal_load_dwordx4 a[124:127], v43, %0 offset:768 nt" :  : "s"(Vb) : "memory","scc","v43","v42","s98","s99");
  pace(tb, (d16 * 28u) >> 4);
  asm volatile("v_xor_b32 v43, 0x43c0000, v45\n\tglobal_load_dwordx4 a[144:147], v43, %0 offset:0 nt\n\tglobal_load_dwordx4 a[148:151], v43, %0 offset:256 nt\n\tglobal_load_dwordx4 a[152:155], v43, %0 offset:512 nt\n\tglobal_load_dwordx4 a[156:159], v43, %0 offset:768 nt" :  : "s"(Vb) : "memory","scc","v43","v42","s98","s99");
  asm volatile("v_mov_b32 v40, 7776\n\ts_mov_b32 vcc_lo, 0x11111111\n\ts_mov_b32 vcc_hi, 0x11111111\n\tv_cndmask_b32 v4, v40, v47, vcc\n\ts_lshl_b64 vcc, vcc, 1\n\tv_cndmask_b32 v5, v40, v47, vcc\n\ts_lshl_b64 vcc, vcc, 1\n\tv_cndmask_b32 v6, v40, v47, vcc\n\ts_lshl_b64 vcc, vcc, 1\n\tv_cndmask_b32 v7, v40, v47, vcc\n\tds_read_b128 v[8:11], v4 offset:8192\n\tds_read_b128 v[12:15], v4 offset:8208\n\tds_read_b128 v[16:19], v5 offset:8192\n\tds_read_b128 v[20:23], v5 offset:8208\n\tds_read_b128 v[24:27], v6 offset:8192\n\tds_read_b128 v[28:31], v6 offset:8208\n\tds_read_b128 v[32:35], v7 offset:8192\n\tds_read_b128 v[36:39], v7 offset:8208\n\ts_waitcnt vmcnt(17)\n\ts_waitcnt lgkmcnt(0)\n\tv_mfma_f32_16x16x32_bf16 v[4:7], a[32:35], v[8:11], 0\n\tv_mfma_f32_16x16x32_bf16 v[40:43], a[40:43], v[8:11], 0\n\tv_mfma_f32_16x16x32_bf16 v[4:7], a[36:39], v[12:15], v[4:7]\n\tv_mfma_f32_16x16x32_bf16 v[40:43], a[44:47], v[12:15], v[40:43]\n\tv_mfma_f32_16x16x32_bf16 v[4:7], a[48:51], v[16:19], v[4:7]\n\tv_mfma_f32_16x16x32_bf16 v[40:43], a[56:59], v[16:19], v[40:43]\n\tv_mfma_f32_16x16x32_bf16 v[4:7], a[52:55], v[20:23], v[4:7]\n\tv_mfma_f32_16x16x32_bf16 v[40:43], a[60:63], v[20:23], v[40:43]\n\tv_mfma_f32_16x16x32_bf16 v[4:7], a[176:179], v[24:27], v[4:7]\n\tv_mfma_f32_16x16x32_bf16 v[40:43], a[184:187], v[24:27], v[40:43]\n\tv_mfma_f32_16x16x32_bf16 v[4:7], a[180:183], v[28:31], v[4:7]\n\tv_mfma_f32_16x16x32_bf16 v[40:43], a[188:191], v[28:31], v[40:43]\n\tv_mfma_f32_16x16x32_bf16 v[4:7], a[64:67], v[32:35], v[4:7]\n\tv_mfma_f32_16x16x32_bf16 v[40:43], a[72:75], v[32:35], v[40:43]\n\tv_mfma_f32_16x16x32_bf16 v[4:7], a[68:71], v[36:39], v[4:7]\n\tv_mfma_f32_16x16x32_bf16 v[40:43], a[76:79], v[36:39], v[40:43]\n\ts_nop 7\n\ts_nop 0\n\tv_add_f32_dpp v8, v5, v4 row_ror:8 row_mask:0xf bank_mask:0x3\n\tv_add_f32_dpp v9, v7, v6 row_ror:8 row_mask:0xf bank_mask:0x3\n\tv_add_f32_dpp v10, v41, v40 row_ror:8 row_mask:0xf bank_mask:0x3\n\tv_add_f32_dpp v11, v43, v42 row_ror:8 row_mask:0xf bank_mask:0x3\n\tv_add_f32_dpp v8, v4, v5 row_ror:8 row_mask:0xf bank_mask:0xc\n\tv_add_f32_dpp v9, v6, v7 row_ror:8 row_mask:0xf bank_mask:0xc\n\tv_add_f32_dpp v10, v40, v41 row_ror:8 row_mask:0xf bank_mask:0xc\n\tv_add_f32_dpp v11, v42, v43 row_ror:8 row_mask:0xf bank_mask:0xc\n\tv_max3_f32 v12, v8, v9, v10\n\tv_max3_f32 v12, v12, v11, v56\n\ts_nop 1\n\tv_max_f32_dpp v12, v12, v12 quad_perm:[1,0,3,2] row_mask:0xf bank_mask:0xf\n\ts_nop 1\n\tv_max_f32_dpp v12, v12, v12 quad_perm:[2,3,0,1] row_mask:0xf bank_mask:0xf\n\tv_mov_b32 v13, v12\n\ts_nop 1\n\tv_permlane16_swap_b32 v12, v13\n\ts_nop 0\n\tv_max_f32 v12, v12, v13\n\tv_mov_b32 v13, v12\n\ts_nop 1\n\tv_permlane32_swap_b32 v12, v13\n\ts_nop 0\n\tv_max_f32 v12, v12, v13\n\tv_mul_f32 v15, %0, v12\n\tv_fma_f32 v13, v56, %0, -v15\n\tv_fma_f32 v8, v8, %0, -v15\n\tv_fma_f32 v9, v9, %0, -v15\n\tv_fma_f32 v10, v10, %0, -v15\n\tv_fma_f32 v11, v11, %0, -v15\n\tv_exp_f32 v13, v13\n\tv_exp_f32 v8, v8\n\tv_exp_f32 v9, v9\n\tv_exp_f32 v10, v10\n\tv_exp_f32 v11, v11\n\tv_mov_b32 v56, v12\n\tv_cvt_pk_bf16_f32 v4, v8, v8\n\tv_cvt_pk_bf16_f32 v5, v9, v9\n\tv_cvt_pk_bf16_f32 v6, v10, v10\n\tv_cvt_pk_bf16_f32 v7, v11, v11\n\tv_add_f32 v8, v8, v9\n\tv_add_f32 v10, v10, v11\n\tv_fma_f32 v57, v57, v13, v8\n\tv_mul_f32 v48, v48, v13\n\tv_mul_f32 v49, v49, v13\n\tv_mul_f32 v50, v50, v13\n\tv_mul_f32 v51, v51, v13\n\tv_add_f32 v57, v57, v10\n\tv_and_b32_dpp v16, v4, v60 quad_perm:[0,0,0,0] row_mask:0xf bank_mask:0xf\n\tv_and_b32_dpp v17, v4, v61 quad_perm:[0,0,0,0] row_mask:0xf bank_mask:0xf\n\tv_and_b32_dpp v18, v4, v62 quad_perm:[0,0,0,0] row_mask:0xf bank_mask:0xf\n\tv_and_b32_dpp v19, v4, v63 quad_perm:[0,0,0,0] row_mask:0xf bank_mask:0xf\n\tv_and_b32_dpp v20, v5, v60 quad_perm:[0,0,0,0] row_mask:0xf bank_mask:0xf\n\tv_and_b32_dpp v21, v5, v61 quad_perm:[0,0,0,0] row_mask:0xf bank_mask:0xf\n\tv_and_b32_dpp v22, v5, v62 quad_perm:[0,0,0,0] row_mask:0xf bank_mask:0xf\n\tv_and_b32_dpp v23, v5, v63 quad_perm:[0,0,0,0] row_mask:0xf bank_mask:0xf\n\ts_waitcnt vmcnt(12)\n\tv_mfma_f32_16x16x32_bf16 v[48:51], a[80:83], v[16:19], v[48:51]\n\tv_and_b32_dpp v16, v6, v60 quad_perm:[0,0,0,0] row_mask:0xf bank_mask:0xf\n\tv_and_b32_dpp v17, v6, v61 quad_perm:[0,0,0,0] row_mask:0xf bank_mask:0xf\n\tv_and_b32_dpp v18, v6, v62 quad_perm:[0,0,0,0] row_mask:0xf bank_mask:0xf\n\tv_and_b32_dpp v19, v6, v63 quad_perm:[0,0,0,0] row_mask:0xf bank_mask:0xf\n\tv_mfma_f32_16x16x32_bf16 v[48:51], a[84:87], v[20:23], v[48:51]\n\tv_and_b32_dpp v20, v7, v60 quad_perm:[0,0,0,0] row_mask:0xf bank_mask:0xf\n\tv_and_b32_dpp v21, v7, v61 quad_perm:[0,0,0,0] row_mask:0xf bank_mask:0xf\n\tv_and_b32_dpp v22, v7, v62 quad_perm:[0,0,0,0] row_mask:0xf bank_mask:0xf\n\tv_and_b32_dpp v23, v7, v63 quad_perm:[0,0,0,0] row_mask:0xf bank_mask:0xf\n\tv_mfma_f32_16x16x32_bf16 v[48:51], a[88:91], v[16:19], v[48:51]\n\tv_and_b32_dpp v16, v4, v60 quad_perm:[1,1,1,1] row_mask:0xf bank_mask:0xf\n\tv_and_b32_dpp v17, v4, v61 quad_perm:[1,1,1,1] row_mask:0xf bank_mask:0xf\n\tv_and_b32_dpp v18, v4, v62 quad_perm:[1,1,1,1] row_mask:0xf bank_mask:0xf\n\tv_and_b32_dpp v19, v4, v63 quad_perm:[1,1,1,1] row_mask:0xf bank_mask:0xf\n\tv_mfma_f32_16x16x32_bf16 v[48:51], a[92:95], v[20:23], v[48:51]\n\tv_and_b32_dpp v20, v5, v60 quad_perm:[1,1,1,1] row_mask:0xf bank_mask:0xf\n\tv_and_b32_dpp v21, v5, v61 quad_perm:[1,1,1,1] row_mask:0xf bank_mask:0xf\n\tv_and_b32_dpp v22, v5, v62 quad_perm:[1,1,1,1] row_mask:0xf bank_mask:0xf\n\tv_and_b32_dpp v23, v5, v63 quad_perm:[1,1,1,1] row_mask:0xf bank_mask:0xf\n\ts_waitcnt vmcnt(8)\n\tv_mfma_f32_16x16x32_bf16 v[48:51], a[96:99], v[16:19], v[48:51]\n\tv_and_b32_dpp v16, v6, v60 quad_perm:[1,1,1,1] row_mask:0xf bank_mask:0xf\n\tv_and_b32_dpp v17, v6, v61 quad_perm:[1,1,1,1] row_mask:0xf bank_mask:0xf\n\tv_and_b32_dpp v18, v6, v62 quad_perm:[1,1,1,1] row_mask:0xf bank_mask:0xf\n\tv_and_b32_dpp v19, v6, v63 quad_perm:[1,1,1,1] row_mask:0xf bank_mask:0xf\n\tv_mfma_f32_16x16x32_bf16 v[48:51], a[100:103], v[20:23], v[48:51]\n\tv_and_b32_dpp v20, v7, v60 quad_perm:[1,1,1,1] row_mask:0xf bank_mask:0xf\n\tv_and_b32_dpp v21, v7, v61 quad_perm:[1,1,1,1] row_mask:0xf bank_mask:0xf\n\tv_and_b32_dpp v22, v7, v62 quad_perm:[1,1,1,1] row_mask:0xf bank_mask:0xf\n\tv_and_b32_dpp v23, v7, v63 quad_perm:[1,1,1,1] row_mask:0xf bank_mask:0xf\n\tv_mfma_f32_16x16x32_bf16 v[48:51], a[104:107], v[16:19], v[48:51]\n\tv_and_b32_dpp v16, v4, v60 quad_perm:[2,2,2,2] row_mask:0xf bank_mask:0xf\n\tv_and_b32_dpp v17, v4, v61 quad_perm:[2,2,2,2] row_mask:0xf bank_mask:0xf\n\tv_and_b32_dpp v18, v4, v62 quad_perm:[2,2,2,2] row_mask:0xf bank_mask:0xf\n\tv_and_b32_dpp v19, v4, v63 quad_perm:[2,2,2,2] row_mask:0xf bank_mask:0xf\n\tv_mfma_f32_16x16x32_bf16 v[48:51], a[108:111], v[20:23], v[48:51]\n\tv_and_b32_dpp v20, v5, v60 quad_perm:[2,2,2,2] row_mask:0xf bank_mask:0xf\n\tv_and_b32_dpp v21, v5, v61 quad_perm:[2,2,2,2] row_mask:0xf bank_mask:0xf\n\tv_and_b32_dpp v22, v5, v62 quad_perm:[2,2,2,2] row_mask:0xf bank_mask:0xf\n\tv_and_b32_dpp v23, v5, v63 quad_perm:[2,2,2,2] row_mask:0xf bank_mask:0xf\n\ts_waitcnt vmcnt(4)\n\tv_mfma_f32_16x16x32_bf16 v[48:51], a[112:115], v[16:19], v[48:51]\n\tv_and_b32_dpp v16, v6, v60 quad_perm:[2,2,2,2] row_mask:0xf bank_mask:0xf\n\tv_and_b32_dpp v17, v6, v61 quad_perm:[2,2,2,2] row_mask:0xf bank_mask:0xf\n\tv_and_b32_dpp v18, v6, v62 quad_perm:[2,2,2,2] row_mask:0xf bank_mask:0xf\n\tv_and_b32_dpp v19, v6, v63 quad_perm:[2,2,2,2] row_mask:0xf bank_mask:0xf\n\tv_mfma_f32_16x16x32_bf16 v[48:51], a[116:119], v[20:23], v[48:51]\n\tv_and_b32_dpp v20, v7, v60 quad_perm:[2,2,2,2] row_mask:0xf bank_mask:0xf\n\tv_and_b32_dpp v21, v7, v61 quad_perm:[2,2,2,2] row_mask:0xf bank_mask:0xf\n\tv_and_b32_dpp v22, v7, v62 quad_perm:[2,2,2,2] row_mask:0xf bank_mask:0xf\n\tv_and_b32_dpp v23, v7, v63 quad_perm:[2,2,2,2] row_mask:0xf bank_mask:0xf\n\tv_mfma_f32_16x16x32_bf16 v[48:51], a[120:123], v[16:19], v[48:51]\n\tv_and_b32_dpp v16, v4, v60 quad_perm:[3,3,3,3] row_mask:0xf bank_mask:0xf\n\tv_and_b32_dpp v17, v4, v61 quad_perm:[3,3,3,3] row_mask:0xf bank_mask:0xf\n\tv_and_b32_dpp v18, v4, v62 quad_perm:[3,3,3,3] row_mask:0xf bank_mask:0xf\n\tv_and_b32_dpp v19, v4, v63 quad_perm:[3,3,3,3] row_mask:0xf bank_mask:0xf\n\tv_mfma_f32_16x16x32_bf16 v[48:51], a[124:127], v[20:23], v[48:51]\n\tv_and_b32_dpp v20, v5, v60 quad_perm:[3,3,3,3] row_mask:0xf bank_mask:0xf\n\tv_and_b32_dpp v21, v5, v61 quad_perm:[3,3,3,3] row_mask:0xf bank_mask:0xf\n\tv_and_b32_dpp v22, v5, v62 quad_perm:[3,3,3,3] row_mask:0xf bank_mask:0xf\n\tv_and_b32_dpp v23, v5, v63 quad_perm:[3,3,3,3] row_mask:0xf bank_mask:0xf\n\tv_mbcnt_lo_u32_b32 v40, -1, 0\n\tv_mbcnt_hi_u32_b32 v40, -1, v40\n\tv_add_f32_dpp v41, v57, v57 quad_perm:[1,0,3,2] row_mask:0xf bank_mask:0xf\n\tv_and_b32 v42, 4, v40\n\tv_lshrrev_b32 v43, 4, v40\n\tv_add_f32_dpp v41, v41, v41 quad_perm:[2,3,0,1] row_mask:0xf bank_mask:0xf\n\tv_add_u32 v42, v42, v43\n\tv_mov_b32 v43, v41\n\tv_lshl_add_u32 v13, v42, 7, %1\n\tv_and_b32 v42, 8, v40\n\tv_permlane16_swap_b32 v41, v43\n\tv_lshl_add_u32 v13, v42, 1, v13\n\tv_add_f32 v41, v41, v43\n\tv_and_b32 v42, 3, v40\n\tv_mov_b32 v43, v41\n\tv_lshl_add_u32 v13, v42, 2, v13\n\tv_lshl_add_u32 v12, v40, 3, %2\n\tv_permlane32_swap_b32 v41, v43\n\tv_mul_f32 v40, %0, v56\n\tv_add_f32 v41, v41, v43\n\ts_mov_b64 exec, 0x11\n\tds_write_b64 v12, v[40:41]\n\ts_mov_b64 exec, -1\n\ts_waitcnt vmcnt(0)\n\tv_mfma_f32_16x16x32_bf16 v[48:51], a[144:147], v[16:19], v[48:51]\n\tv_and_b32_dpp v16, v6, v60 quad_perm:[3,3,3,3] row_mask:0xf bank_mask:0xf\n\tv_and_b32_dpp v17, v6, v61 quad_perm:[3,3,3,3] row_mask:0xf bank_mask:0xf\n\tv_and_b32_dpp v18, v6, v62 quad_perm:[3,3,3,3] row_mask:0xf bank_mask:0xf\n\tv_and_b32_dpp v19, v6, v63 quad_perm:[3,3,3,3] row_mask:0xf bank_mask:0xf\n\tv_mfma_f32_16x16x32_bf16 v[48:51], a[148:151], v[20:23], v[48:51]\n\tv_and_b32_dpp v20, v7, v60 quad_perm:[3,3,3,3] row_mask:0xf bank_mask:0xf\n\tv_and_b32_dpp v21, v7, v61 quad_perm:[3,3,3,3] row_mask:0xf bank_mask:0xf\n\tv_and_b32_dpp v22, v7, v62 quad_perm:[3,3,3,3] row_mask:0xf bank_mask:0xf\n\tv_and_b32_dpp v23, v7, v63 quad_perm:[3,3,3,3] row_mask:0xf bank_mask:0xf\n\tv_mfma_f32_16x16x32_bf16 v[48:51], a[152:155], v[16:19], v[48:51]\n\ts_nop 0\n\tv_mfma_f32_16x16x32_bf16 v[48:51], a[156:159], v[20:23], v[48:51]\n\ts_nop 7\n\ts_nop 0\n\tds_write2_b32 v13, v48, v49 offset1:8\n\tds_write2_b32 v13, v50, v51 offset0:16 offset1:24" :  : "s"(CSC), "s"(MRG + w*1024u), "s"(MLB + w*8u) : "memory","scc","vcc","s98","s99","v4","v5","v6","v7","v8","v9","v10","v11","v12","v13","v14","v15","v16","v17","v18","v19","v20","v21","v22","v23","v24","v25","v26","v27","v28","v29","v30","v31","v32","v33","v34","v35","v36","v37","v38","v39","v40","v41","v42","v43");
  tref = merge_publish(w, s, wsb + 393216ull + (u64)kvh*10240u);
  {
  u32 ln = LANE(); u32 po = (2*xg + (w>>1))*10240u + (w&1)*4096u + gpos(ln>>4, ln&15u); u32 pm = (2*xg + (w>>1))*10240u + 8192u + (ln&15u)*16u + (w&1)*8u;
  u64 pb = wsb + 393216ull; u32x4 a0,a1,a2,a3; u32x2 ml;
  u32 bpa = ln & 48u, rda = RED + ln*16u - (ln>>5)*256u + w*4u; u64 yd = pin(wsb + 761856ull + (u64)(xg*2560 + i32*80));
  asm volatile("" : "+v"(po), "+v"(pm), "+v"(bpa), "+v"(rda));
  pace(tref, 60);
  asm volatile("global_load_dwordx4 %0, %5, %7 offset:0 \n\tglobal_load_dwordx4 %1, %5, %7 offset:1024 \n\tglobal_load_dwordx4 %2, %5, %7 offset:2048 \n\tglobal_load_dwordx4 %3, %5, %7 offset:3072 \n\tglobal_load_dwordx2 %4, %6, %7 \n\ts_waitcnt vmcnt(0)" : "=&v"(a0),"=&v"(a1),"=&v"(a2),"=&v"(a3),"=&v"(ml) : "v"(po), "v"(pm), "s"(pb) : "memory","scc","v43","v42","s98","s99");
  _Pragma("unroll 1") for (int sp = 0; sp < SPIN; sp++) { u32 mx = umax3(umax4(a0), umax4(a1), umax3(umax4(a2), umax4(a3), max(ml[0], ml[1]))); if (!__any(mx == 0xFFFFFFFFu)) break; __builtin_amdgcn_s_sleep(1);
  asm volatile("global_load_dwordx4 %0, %5, %7 offset:0 sc1\n\tglobal_load_dwordx4 %1, %5, %7 offset:1024 sc1\n\tglobal_load_dwordx4 %2, %5, %7 offset:2048 sc1\n\tglobal_load_dwordx4 %3, %5, %7 offset:3072 sc1\n\tglobal_load_dwordx2 %4, %6, %7 sc1\n\ts_waitcnt vmcnt(0)" : "=&v"(a0),"=&v"(a1),"=&v"(a2),"=&v"(a3),"=&v"(ml) : "v"(po), "v"(pm), "s"(pb) : "memory","scc","v43","v42","s98","s99");
  }
  u32 t0_, t1_, t2_, t3_, t4_, t5_;
  asm volatile("s_mov_b32 vcc_lo, 0x30003\n\ts_mov_b32 vcc_hi, 0x30003\n\tv_max_f32_dpp %0, %10, %10 row_ror:8 row_mask:0xf bank_mask:0xf\n\tv_mfma_f32_16x16x32_bf16 v[28:31], %6, a[208:211], 0\n\tv_cndmask_b32 v36, 0, v52, vcc\n\tv_cndmask_b32 v37, 0, v53, vcc\n\tv_max_f32_dpp %0, %0, %0 row_ror:4 row_mask:0xf bank_mask:0xf\n\tv_cndmask_b32 v38, 0, v54, vcc\n\tv_cndmask_b32 v39, 0, v55, vcc\n\ts_lshl_b64 vcc, vcc, 2\n\tv_max_f32_dpp %0, %0, %0 row_ror:2 row_mask:0xf bank_mask:0xf\n\tv_cndmask_b32 v40, 0, v52, vcc\n\tv_cndmask_b32 v41, 0, v53, vcc\n\tv_max_f32_dpp %0, %0, %0 row_ror:1 row_mask:0xf bank_mask:0xf\n\tv_mfma_f32_16x16x32_bf16 v[32:35], %6, v[36:39], 0\n\tv_sub_f32 %0, %10, %0\n\tv_cndmask_b32 v42, 0, v54, vcc\n\tv_exp_f32 %0, %0\n\tv_cndmask_b32 v43, 0, v55, vcc\n\ts_lshl_b64 vcc, vcc, 2\n\tv_mfma_f32_16x16x32_bf16 v[28:31], %7, a[212:215], v[28:31]\n\tv_mul_f32 %0, %0, %11\n\tv_cndmask_b32 v36, 0, v52, vcc\n\tds_bpermute_b32 %2, %12, %0 offset:0\n\tds_bpermute_b32 %3, %12, %0 offset:4\n\tds_bpermute_b32 %4, %12, %0 offset:8\n\tds_bpermute_b32 %5, %12, %0 offset:12\n\tv_mfma_f32_16x16x32_bf16 v[32:35], %7, v[40:43], v[32:35]\n\tv_add_f32_dpp %1, %0, %0 row_ror:8 row_mask:0xf bank_mask:0xf\n\tv_cndmask_b32 v37, 0, v53, vcc\n\tv_cndmask_b32 v38, 0, v54, vcc\n\tv_add_f32_dpp %1, %1, %1 row_ror:4 row_mask:0xf bank_mask:0xf\n\tv_cndmask_b32 v39, 0, v55, vcc\n\ts_lshl_b64 vcc, vcc, 2\n\tv_mfma_f32_16x16x32_bf16 v[28:31], %8, a[216:219], v[28:31]\n\tv_add_f32_dpp %1, %1, %1 row_ror:2 row_mask:0xf bank_mask:0xf\n\tv_cndmask_b32 v40, 0, v52, vcc\n\tv_mfma_f32_16x16x32_bf16 v[32:35], %8, v[36:39], v[32:35]\n\tv_add_f32_dpp %1, %1, %1 row_ror:1 row_mask:0xf bank_mask:0xf\n\tv_cndmask_b32 v41, 0, v53, vcc\n\tv_cndmask_b32 v42, 0, v54, vcc\n\tv_cndmask_b32 v43, 0, v55, vcc\n\tv_mfma_f32_16x16x32_bf16 v[28:31], %9, a[220:223], v[28:31]\n\tv_rcp_f32 %1, %1\n\tv_mfma_f32_16x16x32_bf16 v[32:35], %9, v[40:43], v[32:35]\n\ts_nop 5\n\ts_waitcnt lgkmcnt(0)\n\tv_mul_f32 %0, %2, v28\n\tv_fmac_f32 %0, %3, v29\n\tv_fmac_f32 %0, %4, v30\n\tv_fmac_f32 %0, %5, v31\n\tv_mul_f32 %2, %2, v32\n\tv_fmac_f32 %2, %3, v33\n\tv_fmac_f32 %2, %4, v34\n\tv_fmac_f32 %2, %5, v35\n\ts_nop 1\n\tv_permlane32_swap_b32 %0, %2\n\ts_nop 0\n\tv_add_f32 %0, %0, %2\n\tv_mov_b32 %2, %0\n\ts_nop 1\n\tv_permlane16_swap_b32 %0, %2\n\ts_nop 0\n\tv_add_f32 %0, %0, %2\n\ts_nop 1\n\tv_add_f32_dpp %0, %0, %0 row_shl:2 row_mask:0x4 bank_mask:0xf\n\ts_nop 1\n\tv_add_f32_dpp %0, %0, %0 row_shl:4 row_mask:0x4 bank_mask:0xf\n\ts_mov_b32 exec_lo, 0xffff\n\ts_mov_b32 exec_hi, 3\n\tv_mul_f32 %0, %0, %1\n\tds_write_b32 %13, %0\n\ts_mov_b64 exec, -1\n\ts_waitcnt lgkmcnt(0)\n\ts_barrier" : "=&v"(t0_),"=&v"(t1_),"=&v"(t2_),"=&v"(t3_),"=&v"(t4_),"=&v"(t5_) : "v"(a0),"v"(a1),"v"(a2),"v"(a3),"v"(ml[0]),"v"(ml[1]),"v"(bpa),"v"(rda) : "memory","scc","vcc","v28","v29","v30","v31","v32","v33","v34","v35","v36","v37","v38","v39","v40","v41","v42","v43");
  tref = now32(); wo_finish(ln, w, yd); }
  {
  u32 ln = LANE(); u32 l40 = min(ln, 39u); u32 yo = w*640u + l40*16u; u64 yb = wsb + 761856ull + 2560ull, yb2 = yb + 7680ull, yb3 = yb + 15360ull;
  u32 xa = 10368u + (8u*w + l40/5u)*36u + (l40%5u)*8u; u32 x01 = *LDSP(u32, xa), x23 = *LDSP(u32, xa + 4);
  u32x4 y0,y1,y2,y3,y4,y5,y6,y7; u32 xb = l40 % 5u != 4u ? xa + 4u : ONES + 128u; f32x2 x0 = up2(x01), x2 = up2(x23);
  yb = pin(yb); yb2 = pin(yb2); yb3 = pin(yb3); asm volatile("" : "+v"(yo), "+v"(xa), "+v"(xb), "+v"(x0), "+v"(x2));
  pace(tref, 60);
  asm volatile("global_load_dwordx4 %0, %8, %9 offset:-2560 \n\tglobal_load_dwordx4 %1, %8, %9 offset:0 \n\tglobal_load_dwordx4 %2, %8, %9 offset:2560 \n\tglobal_load_dwordx4 %3, %8, %10 offset:-2560 \n\tglobal_load_dwordx4 %4, %8, %10 offset:0 \n\tglobal_load_dwordx4 %5, %8, %10 offset:2560 \n\tglobal_load_dwordx4 %6, %8, %11 offset:-2560 \n\tglobal_load_dwordx4 %7, %8, %11 offset:0 \n\ts_waitcnt vmcnt(0)" : "=&v"(y0),"=&v"(y1),"=&v"(y2),"=&v"(y3),"=&v"(y4),"=&v"(y5),"=&v"(y6),"=&v"(y7) : "v"(yo), "s"(yb), "s"(yb2), "s"(yb3) : "memory","scc","v43","v42","s98","s99");
  f32x4 ys = ((F4(y0)+F4(y1))+(F4(y2)+F4(y3))) + ((F4(y4)+F4(y5))+(F4(y6)+F4(y7))); if (__any(__builtin_isunordered(ys[0], ys[1]) || __builtin_isunordered(ys[2], ys[3]))) {
  _Pragma("unroll 1") for (int sp = 0; sp < SPIN; sp++) {
  asm volatile("global_load_dwordx4 %0, %8, %9 offset:-2560 sc1\n\tglobal_load_dwordx4 %1, %8, %9 offset:0 sc1\n\tglobal_load_dwordx4 %2, %8, %9 offset:2560 sc1\n\tglobal_load_dwordx4 %3, %8, %10 offset:-2560 sc1\n\tglobal_load_dwordx4 %4, %8, %10 offset:0 sc1\n\tglobal_load_dwordx4 %5, %8, %10 offset:2560 sc1\n\tglobal_load_dwordx4 %6, %8, %11 offset:-2560 sc1\n\tglobal_load_dwordx4 %7, %8, %11 offset:0 sc1\n\ts_waitcnt vmcnt(0)" : "=&v"(y0),"=&v"(y1),"=&v"(y2),"=&v"(y3),"=&v"(y4),"=&v"(y5),"=&v"(y6),"=&v"(y7) : "v"(yo), "s"(yb), "s"(yb2), "s"(yb3) : "memory","scc","v43","v42","s98","s99");
  u32 mx = umax3(umax3(umax4(y0), umax4(y1), umax4(y2)), umax3(umax4(y3), umax4(y4), umax4(y5)), max(umax4(y6), umax4(y7))); if (!__any(mx == 0xFFFFFFFFu)) break; __builtin_amdgcn_s_sleep(1); }
  ys = ((F4(y0)+F4(y1))+(F4(y2)+F4(y3))) + ((F4(y4)+F4(y5))+(F4(y6)+F4(y7))); }
  *LDSP(u32, xa) = pkv(x0 + f32x2{ys[0], ys[1]}); *LDSP(u32, xb) = pkv(x2 + f32x2{ys[2], ys[3]});
  BAR(); }
  { u32 ln = LANE(); u32x4 xp; u32 xt;
  u32 ha = (ln&1u) ? ONES : 11520u + (s>>2)*288u + (ln>>4)*72u; u64 qd = pin(wsb + 868352ull + (u64)(kvh*4096 + (s&3)*1024 + w*256 + (s>>2)*4));
  asm volatile("" : "+v"(ha));
  mixer(10368u, ln, xp, xt);
  *LDSP(u32x4, MRG + ln*16u) = xp; *LDSP(u16, MRG + 1024u + ln*2u) = (u16)xt;
  u32x4 f0 = ld8x2(ha), f1 = ld8x2(ha+16), f2 = ld8x2(ha+32), f3 = ld8x2(ha+48); u32x2 f4 = *LDSP(u32x2, ha+64); f32x4 dq, de; Cen c = stats1(xp, xt);
  asm volatile("s_nop 1\n\tv_mfma_f32_16x16x32_bf16 %0, %4, a[224:227], 0\n\tv_mfma_f32_16x16x32_bf16 %0, %5, a[228:231], %0\n\tv_mfma_f32_16x16x32_bf16 %0, %6, a[232:235], %0\n\tv_mfma_f32_16x16x32_bf16 %0, %7, a[236:239], %0\n\tv_mfma_f32_16x16x16_bf16 %1, %8, v[58:59], 0" : "=&v"(dq), "=&v"(de), "+v"(c.s1), "+v"(c.s2) : "v"(f0),"v"(f1),"v"(f2),"v"(f3),"v"(f4) : "memory");
  stats2(c); float rs = __builtin_amdgcn_rsqf(c.var + 1e-5f), mn = c.pv + c.md; asm volatile("" : "+v"(rs), "+v"(mn), "+v"(dq), "+v"(de));
  if (ln < 16u) st4(ln*16u, nz(__float_as_uint(fmaf(-mn, dq[1] + de[1], dq[0] + de[0]) * rs)), qd);
  xsave = xp; xsave_t = xt; }
  asm volatile("ds_read_b128 a[240:243], v46 offset:12288\n\tds_read_b128 a[244:247], v46 offset:13312\n\tds_read_b128 a[248:251], v46 offset:14336\n\tds_read_b128 a[252:255], v46 offset:15360\n\tds_read_b128 a[160:163], v46 offset:16384\n\tds_read_b128 a[164:167], v46 offset:17408\n\tds_read_b128 a[168:171], v46 offset:18432\n\tds_read_b128 a[172:175], v46 offset:19456\n\tds_read_b128 a[192:195], v46 offset:20480\n\tds_read_b128 a[196:199], v46 offset:21504\n\tds_read_b128 a[200:203], v46 offset:22528\n\tds_read_b128 a[204:207], v46 offset:23552\n\ts_waitcnt lgkmcnt(0)" :  :  : "memory");
  {
  u32 ln = LANE(); u32 qo = ln*16u; u64 qb_ = wsb + 868352ull + (u64)(kvh*4096 + w*1024); u32x4 a0;
  delay(25);
  _Pragma("unroll 1") for (int sp = 0; sp < SPIN; sp++) {
  if (sp) { asm volatile("global_load_dwordx4 %0, %1, %2 sc1\n\ts_waitcnt vmcnt(0)" : "=&v"(a0) : "v"(qo), "s"(qb_) : "memory"); } else { asm volatile("global_load_dwordx4 %0, %1, %2 \n\ts_waitcnt vmcnt(0)" : "=&v"(a0) : "v"(qo), "s"(qb_) : "memory","scc","v43","v42","s98","s99"); }
  if (!__any(umax4(a0) == 0xFFFFFFFFu)) break; __builtin_amdgcn_s_sleep(1); }
  f32x4 v = F4(a0); *LDSP(u16, 8704u + qpos(w, ln)) = (u16)pk2((v[0]+v[1])+(v[2]+v[3]), 0.f);
  BAR();
  if (w == 3) { *LDSP(u32x4, 10368u + ln*16u) = xsave; *LDSP(u16, 11392u + ln*2u) = (u16)xsave_t; } }
  asm volatile("v_mov_b32 v40, 7264\n\ts_mov_b32 vcc_lo, 0x11111111\n\ts_mov_b32 vcc_hi, 0x11111111\n\tv_cndmask_b32 v4, v40, v47, vcc\n\ts_lshl_b64 vcc, vcc, 1\n\tv_cndmask_b32 v5, v40, v47, vcc\n\ts_lshl_b64 vcc, vcc, 1\n\tv_cndmask_b32 v6, v40, v47, vcc\n\ts_lshl_b64 vcc, vcc, 1\n\tv_cndmask_b32 v7, v40, v47, vcc\n\tds_read_b128 v[8:11], v4 offset:8704\n\tds_read_b128 v[12:15], v4 offset:8720\n\tds_read_b128 v[16:19], v5 offset:8704\n\tds_read_b128 v[20:23], v5 offset:8720\n\tds_read_b128 v[24:27], v6 offset:8704\n\tds_read_b128 v[28:31], v6 offset:8720\n\tds_read_b128 v[32:35], v7 offset:8704\n\tds_read_b128 v[36:39], v7 offset:8720\n\ts_waitcnt lgkmcnt(6)\n\tv_mfma_f32_16x16x32_bf16 v[4:7], v[240:243], v[8:11], 0\n\tv_mfma_f32_16x16x32_bf16 v[40:43], v[248:251], v[8:11], 0\n\tv_mfma_f32_16x16x32_bf16 v[4:7], v[244:247], v[12:15], v[4:7]\n\tv_mfma_f32_16x16x32_bf16 v[40:43], v[252:255], v[12:15], v[40:43]\n\ts_waitcnt lgkmcnt(4)\n\tv_mfma_f32_16x16x32_bf16 v[4:7], v[128:131], v[16:19], v[4:7]\n\tv_mfma_f32_16x16x32_bf16 v[40:43], v[136:139], v[16:19], v[40:43]\n\tv_mfma_f32_16x16x32_bf16 v[4:7], v[132:135], v[20:23], v[4:7]\n\tv_mfma_f32_16x16x32_bf16 v[40:43], v[140:143], v[20:23], v[40:43]\n\tds_read_b128 v[128:131], v46 offset:24576\n\tds_read_b128 v[132:135], v46 offset:25600\n\tds_read_b128 v[136:139], v46 offset:26624\n\tds_read_b128 v[140:143], v46 offset:27648\n\ts_waitcnt lgkmcnt(6)\n\tv_mfma_f32_16x16x32_bf16 v[4:7], v[144:147], v[24:27], v[4:7]\n\tv_mfma_f32_16x16x32_bf16 v[40:43], v[152:155], v[24:27], v[40:43]\n\tv_mfma_f32_16x16x32_bf16 v[4:7], v[148:151], v[28:31], v[4:7]\n\tv_mfma_f32_16x16x32_bf16 v[40:43], v[156:159], v[28:31], v[40:43]\n\tds_read_b128 v[144:147], v46 offset:28672\n\tds_read_b128 v[148:151], v46 offset:29696\n\tds_read_b128 v[152:155], v46 offset:30720\n\tds_read_b128 v[156:159], v46 offset:31744\n\ts_waitcnt lgkmcnt(8)\n\tv_mfma_f32_16x16x32_bf16 v[4:7], a[240:243], v[32:35], v[4:7]\n\tv_mfma_f32_16x16x32_bf16 v[40:43], a[248:251], v[32:35], v[40:43]\n\tv_mfma_f32_16x16x32_bf16 v[4:7], a[244:247], v[36:39], v[4:7]\n\tv_mfma_f32_16x16x32_bf16 v[40:43], a[252:255], v[36:39], v[40:43]\n\tds_read_b128 a[240:243], v46 offset:32768\n\tds_read_b128 a[244:247], v46 offset:33792\n\tds_read_b128 a[248:251], v46 offset:34816\n\tds_read_b128 a[252:255], v46 offset:35840\n\tv_mfma_f32_16x16x32_bf16 v[248:251], a[160:163], v[8:11], 0\n\tv_mfma_f32_16x16x32_bf16 v[252:255], a[168:171], v[8:11], 0\n\tv_mfma_f32_16x16x32_bf16 v[248:251], a[164:167], v[12:15], v[248:251]\n\tv_mfma_f32_16x16x32_bf16 v[252:255], a[172:175], v[12:15], v[252:255]\n\tds_read_b128 a[160:163], v46 offset:0\n\tds_read_b128 a[164:167], v46 offset:1024\n\tds_read_b128 a[168:171], v46 offset:2048\n\tds_read_b128 a[172:175], v46 offset:3072\n\tv_mfma_f32_16x16x32_bf16 v[248:251], a[192:195], v[16:19], v[248:251]\n\tv_add_f32_dpp v240, v5, v4 row_ror:8 row_mask:0xf bank_mask:0x3\n\tv_mfma_f32_16x16x32_bf16 v[252:255], a[200:203], v[16:19], v[252:255]\n\tv_add_f32_dpp v241, v7, v6 row_ror:8 row_mask:0xf bank_mask:0x3\n\tv_mfma_f32_16x16x32_bf16 v[248:251], a[196:199], v[20:23], v[248:251]\n\tv_add_f32_dpp v242, v41, v40 row_ror:8 row_mask:0xf bank_mask:0x3\n\tv_mfma_f32_16x16x32_bf16 v[252:255], a[204:207], v[20:23], v[252:255]\n\tv_add_f32_dpp v243, v43, v42 row_ror:8 row_mask:0xf bank_mask:0x3\n\tds_read_b128 a[192:195], v46 offset:4096\n\tds_read_b128 a[196:199], v46 offset:5120\n\tds_read_b128 a[200:203], v46 offset:6144\n\tds_read_b128 a[204:207], v46 offset:7168\n\tv_mfma_f32_16x16x32_bf16 v[248:251], a[32:35], v[24:27], v[248:251]\n\tv_add_f32_dpp v240, v4, v5 row_ror:8 row_mask:0xf bank_mask:0xc\n\tv_mfma_f32_16x16x32_bf16 v[252:255], a[40:43], v[24:27], v[252:255]\n\tv_add_f32_dpp v241, v6, v7 row_ror:8 row_mask:0xf bank_mask:0xc\n\tv_mfma_f32_16x16x32_bf16 v[248:251], a[36:39], v[28:31], v[248:251]\n\tv_add_f32_dpp v242, v40, v41 row_ror:8 row_mask:0xf bank_mask:0xc\n\tv_mfma_f32_16x16x32_bf16 v[252:255], a[44:47], v[28:31], v[252:255]\n\tv_add_f32_dpp v243, v42, v43 row_ror:8 row_mask:0xf bank_mask:0xc\n\tds_read_b128 a[32:35], v46 offset:8192\n\tds_read_b128 a[36:39], v46 offset:9216\n\tds_read_b128 a[40:43], v46 offset:10240\n\tds_read_b128 a[44:47], v46 offset:11264\n\tv_mfma_f32_16x16x32_bf16 v[248:251], a[48:51], v[32:35], v[248:251]\n\tv_max3_f32 v56, v240, v241, v242\n\tv_mfma_f32_16x16x32_bf16 v[252:255], a[56:59], v[32:35], v[252:255]\n\tv_max_f32 v56, v56, v243\n\tv_mfma_f32_16x16x32_bf16 v[248:251], a[52:55], v[36:39], v[248:251]\n\tv_mfma_f32_16x16x32_bf16 v[252:255], a[60:63], v[36:39], v[252:255]\n\tv_mfma_f32_16x16x32_bf16 v[4:7], a[176:179], v[8:11], 0\n\tv_mfma_f32_16x16x32_bf16 v[40:43], a[184:187], v[8:11], 0\n\tv_mfma_f32_16x16x32_bf16 v[4:7], a[180:183], v[12:15], v[4:7]\n\tv_mfma_f32_16x16x32_bf16 v[40:43], a[188:191], v[12:15], v[40:43]\n\tv_mfma_f32_16x16x32_bf16 v[4:7], a[64:67], v[16:19], v[4:7]\n\tv_add_f32_dpp v244, v249, v248 row_ror:8 row_mask:0xf bank_mask:0x3\n\tv_mfma_f32_16x16x32_bf16 v[40:43], a[72:75], v[16:19], v[40:43]\n\tv_add_f32_dpp v245, v251, v250 row_ror:8 row_mask:0xf bank_mask:0x3\n\tv_mfma_f32_16x16x32_bf16 v[4:7], a[68:71], v[20:23], v[4:7]\n\tv_add_f32_dpp v246, v253, v252 row_ror:8 row_mask:0xf bank_mask:0x3\n\tv_mfma_f32_16x16x32_bf16 v[40:43], a[76:79], v[20:23], v[40:43]\n\tv_add_f32_dpp v247, v255, v254 row_ror:8 row_mask:0xf bank_mask:0x3\n\ts_waitcnt lgkmcnt(15)\n\tv_mfma_f32_16x16x32_bf16 v[4:7], v[128:131], v[24:27], v[4:7]\n\tv_add_f32_dpp v244, v248, v249 row_ror:8 row_mask:0xf bank_mask:0xc\n\tv_mfma_f32_16x16x32_bf16 v[40:43], v[136:139], v[24:27], v[40:43]\n\tv_add_f32_dpp v245, v250, v251 row_ror:8 row_mask:0xf bank_mask:0xc\n\tv_mfma_f32_16x16x32_bf16 v[4:7], v[132:135], v[28:31], v[4:7]\n\tv_add_f32_dpp v246, v252, v253 row_ror:8 row_mask:0xf bank_mask:0xc\n\tv_mfma_f32_16x16x32_bf16 v[40:43], v[140:143], v[28:31], v[40:43]\n\tv_add_f32_dpp v247, v254, v255 row_ror:8 row_mask:0xf bank_mask:0xc\n\ts_waitcnt lgkmcnt(15)\n\tv_mfma_f32_16x16x32_bf16 v[4:7], v[144:147], v[32:35], v[4:7]\n\tv_max3_f32 v56, v244, v245, v56\n\tv_mfma_f32_16x16x32_bf16 v[40:43], v[152:155], v[32:35], v[40:43]\n\tv_max3_f32 v56, v246, v247, v56\n\tv_mfma_f32_16x16x32_bf16 v[4:7], v[148:151], v[36:39], v[4:7]\n\tv_mfma_f32_16x16x32_bf16 v[40:43], v[156:159], v[36:39], v[40:43]\n\ts_waitcnt lgkmcnt(12)\n\tv_mfma_f32_16x16x32_bf16 v[248:251], a[240:243], v[8:11], 0\n\tv_mfma_f32_16x16x32_bf16 v[252:255], a[248:251], v[8:11], 0\n\tv_mfma_f32_16x16x32_bf16 v[248:251], a[244:247], v[12:15], v[248:251]\n\tv_mfma_f32_16x16x32_bf16 v[252:255], a[252:255], v[12:15], v[252:255]\n\ts_waitcnt lgkmcnt(8)\n\tv_mfma_f32_16x16x32_bf16 v[248:251], a[160:163], v[16:19], v[248:251]\n\tv_add_f32_dpp v48, v5, v4 row_ror:8 row_mask:0xf bank_mask:0x3\n\tv_mfma_f32_16x16x32_bf16 v[252:255], a[168:171], v[16:19], v[252:255]\n\tv_add_f32_dpp v49, v7, v6 row_ror:8 row_mask:0xf bank_mask:0x3\n\tv_mfma_f32_16x16x32_bf16 v[248:251], a[164:167], v[20:23], v[248:251]\n\tv_add_f32_dpp v50, v41, v40 row_ror:8 row_mask:0xf bank_mask:0x3\n\tv_mfma_f32_16x16x32_bf16 v[252:255], a[172:175], v[20:23], v[252:255]\n\tv_add_f32_dpp v51, v43, v42 row_ror:8 row_mask:0xf bank_mask:0x3\n\ts_waitcnt lgkmcnt(4)\n\tv_mfma_f32_16x16x32_bf16 v[248:251], a[192:195], v[24:27], v[248:251]\n\tv_add_f32_dpp v48, v4, v5 row_ror:8 row_mask:0xf bank_mask:0xc\n\tv_mfma_f32_16x16x32_bf16 v[252:255], a[200:203], v[24:27], v[252:255]\n\tv_add_f32_dpp v49, v6, v7 row_ror:8 row_mask:0xf bank_mask:0xc\n\tv_mfma_f32_16x16x32_bf16 v[248:251], a[196:199], v[28:31], v[248:251]\n\tv_add_f32_dpp v50, v40, v41 row_ror:8 row_mask:0xf bank_mask:0xc\n\tv_mfma_f32_16x16x32_bf16 v[252:255], a[204:207], v[28:31], v[252:255]\n\tv_add_f32_dpp v51, v42, v43 row_ror:8 row_mask:0xf bank_mask:0xc\n\ts_waitcnt lgkmcnt(0)\n\tv_mfma_f32_16x16x32_bf16 v[248:251], a[32:35], v[32:35], v[248:251]\n\tv_max3_f32 v56, v48, v49, v56\n\tv_mfma_f32_16x16x32_bf16 v[252:255], a[40:43], v[32:35], v[252:255]\n\tv_max3_f32 v56, v50, v51, v56\n\tv_mfma_f32_16x16x32_bf16 v[248:251], a[36:39], v[36:39], v[248:251]\n\tv_mfma_f32_16x16x32_bf16 v[252:255], a[44:47], v[36:39], v[252:255]\n\ts_nop 7\n\ts_nop 1\n\tv_add_f32_dpp v8, v249, v248 row_ror:8 row_mask:0xf bank_mask:0x3\n\tv_add_f32_dpp v9, v251, v250 row_ror:8 row_mask:0xf bank_mask:0x3\n\tv_add_f32_dpp v10, v253, v252 row_ror:8 row_mask:0xf bank_mask:0x3\n\tv_add_f32_dpp v11, v255, v254 row_ror:8 row_mask:0xf bank_mask:0x3\n\tv_add_f32_dpp v8, v248, v249 row_ror:8 row_mask:0xf bank_mask:0xc\n\tv_add_f32_dpp v9, v250, v251 row_ror:8 row_mask:0xf bank_mask:0xc\n\tv_add_f32_dpp v10, v252, v253 row_ror:8 row_mask:0xf bank_mask:0xc\n\tv_add_f32_dpp v11, v254, v255 row_ror:8 row_mask:0xf bank_mask:0xc\n\tv_max3_f32 v56, v8, v9, v56\n\tv_max3_f32 v56, v10, v11, v56\n\ts_nop 1\n\tv_max_f32_dpp v56, v56, v56 quad_perm:[1,0,3,2] row_mask:0xf bank_mask:0xf\n\ts_nop 1\n\tv_max_f32_dpp v56, v56, v56 quad_perm:[2,3,0,1] row_mask:0xf bank_mask:0xf\n\tv_mov_b32 v13, v56\n\ts_nop 1\n\tv_permlane16_swap_b32 v56, v13\n\ts_nop 0\n\tv_max_f32 v56, v56, v13\n\tv_mov_b32 v13, v56\n\ts_nop 1\n\tv_permlane32_swap_b32 v56, v13\n\ts_nop 0\n\tv_max_f32 v56, v56, v13\n\tv_mul_f32 v15, %0, v56\n\tv_fma_f32 v240, v240, %0, -v15\n\tv_fma_f32 v241, v241, %0, -v15\n\tv_fma_f32 v242, v242, %0, -v15\n\tv_fma_f32 v243, v243, %0, -v15\n\tv_fma_f32 v244, v244, %0, -v15\n\tv_fma_f32 v245, v245, %0, -v15\n\tv_fma_f32 v246, v246, %0, -v15\n\tv_fma_f32 v247, v247, %0, -v15\n\tv_fma_f32 v48, v48, %0, -v15\n\tv_fma_f32 v49, v49, %0, -v15\n\tv_fma_f32 v50, v50, %0, -v15\n\tv_fma_f32 v51, v51, %0, -v15\n\tv_fma_f32 v8, v8, %0, -v15\n\tv_fma_f32 v9, v9, %0, -v15\n\tv_fma_f32 v10, v10, %0, -v15\n\tv_fma_f32 v11, v11, %0, -v15\n\tv_exp_f32 v240, v240\n\tv_exp_f32 v241, v241\n\tv_exp_f32 v242, v242\n\tv_exp_f32 v243, v243\n\tv_exp_f32 v244, v244\n\tv_exp_f32 v245, v245\n\tv_exp_f32 v246, v246\n\tv_exp_f32 v247, v247\n\tv_exp_f32 v48, v48\n\tv_exp_f32 v49, v49\n\tv_exp_f32 v50, v50\n\tv_exp_f32 v51, v51\n\tv_exp_f32 v8, v8\n\tv_exp_f32 v9, v9\n\tv_exp_f32 v10, v10\n\tv_exp_f32 v11, v11\n\tv_cvt_pk_bf16_f32 v24, v240, v240\n\tv_cvt_pk_bf16_f32 v25, v241, v241\n\tv_cvt_pk_bf16_f32 v26, v242, v242\n\tv_cvt_pk_bf16_f32 v27, v243, v243\n\tv_cvt_pk_bf16_f32 v28, v244, v244\n\tv_cvt_pk_bf16_f32 v29, v245, v245\n\tv_cvt_pk_bf16_f32 v30, v246, v246\n\tv_cvt_pk_bf16_f32 v31, v247, v247\n\tv_cvt_pk_bf16_f32 v32, v48, v48\n\tv_cvt_pk_bf16_f32 v33, v49, v49\n\tv_cvt_pk_bf16_f32 v34, v50, v50\n\tv_cvt_pk_bf16_f32 v35, v51, v51\n\tv_cvt_pk_bf16_f32 v36, v8, v8\n\tv_cvt_pk_bf16_f32 v37, v9, v9\n\tv_cvt_pk_bf16_f32 v38, v10, v10\n\tv_cvt_pk_bf16_f32 v39, v11, v11\n\tv_pk_add_f32 v[240:241], v[240:241], v[242:243]\n\tv_pk_add_f32 v[244:245], v[244:245], v[246:247]\n\tv_pk_add_f32 v[48:49], v[48:49], v[50:51]\n\tv_pk_add_f32 v[8:9], v[8:9], v[10:11]\n\tv_pk_add_f32 v[240:241], v[240:241], v[244:245]\n\tv_pk_add_f32 v[8:9], v[8:9], v[48:49]\n\tv_pk_add_f32 v[8:9], v[8:9], v[240:241]\n\tv_add_f32 v57, v8, v9\n\tv_mbcnt_lo_u32_b32 v40, -1, 0\n\tv_mbcnt_hi_u32_b32 v40, -1, v40\n\tv_add_f32_dpp v41, v57, v57 quad_perm:[1,0,3,2] row_mask:0xf bank_mask:0xf\n\tv_and_b32 v42, 4, v40\n\tv_lshrrev_b32 v43, 4, v40\n\tv_add_f32_dpp v41, v41, v41 quad_perm:[2,3,0,1] row_mask:0xf bank_mask:0xf\n\tv_add_u32 v42, v42, v43\n\tv_mov_b32 v43, v41\n\tv_lshl_add_u32 v13, v42, 7, %1\n\tv_and_b32 v42, 8, v40\n\tv_permlane16_swap_b32 v41, v43\n\tv_lshl_add_u32 v13, v42, 1, v13\n\tv_add_f32 v41, v41, v43\n\tv_and_b32 v42, 3, v40\n\tv_mov_b32 v43, v41\n\tv_lshl_add_u32 v13, v42, 2, v13\n\tv_lshl_add_u32 v12, v40, 3, %2\n\tv_permlane32_swap_b32 v41, v43\n\tv_mul_f32 v40, %0, v56\n\tv_add_f32 v41, v41, v43\n\ts_mov_b64 exec, 0x11\n\tds_write_b64 v12, v[40:41]\n\ts_mov_b64 exec, -1\n\tv_and_b32_dpp v16, v24, v60 quad_perm:[0,0,0,0] row_mask:0xf bank_mask:0xf\n\tv_and_b32_dpp v17, v24, v61 quad_perm:[0,0,0,0] row_mask:0xf bank_mask:0xf\n\tv_and_b32_dpp v18, v24, v62 quad_perm:[0,0,0,0] row_mask:0xf bank_mask:0xf\n\tv_and_b32_dpp v19, v24, v63 quad_perm:[0,0,0,0] row_mask:0xf bank_mask:0xf\n\tv_and_b32_dpp v20, v25, v60 quad_perm:[0,0,0,0] row_mask:0xf bank_mask:0xf\n\tv_and_b32_dpp v21, v25, v61 quad_perm:[0,0,0,0] row_mask:0xf bank_mask:0xf\n\tv_and_b32_dpp v22, v25, v62 quad_perm:[0,0,0,0] row_mask:0xf bank_mask:0xf\n\tv_and_b32_dpp v23, v25, v63 quad_perm:[0,0,0,0] row_mask:0xf bank_mask:0xf\n\tv_mfma_f32_16x16x32_bf16 v[48:51], v[80:83], v[16:19], 0\n\tv_and_b32_dpp v16, v26, v60 quad_perm:[0,0,0,0] row_mask:0xf bank_mask:0xf\n\tv_and_b32_dpp v17, v26, v61 quad_perm:[0,0,0,0] row_mask:0xf bank_mask:0xf\n\tv_and_b32_dpp v18, v26, v62 quad_perm:[0,0,0,0] row_mask:0xf bank_mask:0xf\n\tv_and_b32_dpp v19, v26, v63 quad_perm:[0,0,0,0] row_mask:0xf bank_mask:0xf\n\tv_mfma_f32_16x16x32_bf16 v[48:51], v[84:87], v[20:23], v[48:51]\n\tv_and_b32_dpp v20, v27, v60 quad_perm:[0,0,0,0] row_mask:0xf bank_mask:0xf\n\tv_and_b32_dpp v21, v27, v61 quad_perm:[0,0,0,0] row_mask:0xf bank_mask:0xf\n\tv_and_b32_dpp v22, v27, v62 quad_perm:[0,0,0,0] row_mask:0xf bank_mask:0xf\n\tv_and_b32_dpp v23, v27, v63 quad_perm:[0,0,0,0] row_mask:0xf bank_mask:0xf\n\tv_mfma_f32_16x16x32_bf16 v[48:51], v[88:91], v[16:19], v[48:51]\n\tv_and_b32_dpp v16, v24, v60 quad_perm:[1,1,1,1] row_mask:0xf bank_mask:0xf\n\tv_and_b32_dpp v17, v24, v61 quad_perm:[1,1,1,1] row_mask:0xf bank_mask:0xf\n\tv_and_b32_dpp v18, v24, v62 quad_perm:[1,1,1,1] row_mask:0xf bank_mask:0xf\n\tv_and_b32_dpp v19, v24, v63 quad_perm:[1,1,1,1] row_mask:0xf bank_mask:0xf\n\tv_mfma_f32_16x16x32_bf16 v[48:51], v[92:95], v[20:23], v[48:51]\n\tv_and_b32_dpp v20, v25, v60 quad_perm:[1,1,1,1] row_mask:0xf bank_mask:0xf\n\tv_and_b32_dpp v21, v25, v61 quad_perm:[1,1,1,1] row_mask:0xf bank_mask:0xf\n\tv_and_b32_dpp v22, v25, v62 quad_perm:[1,1,1,1] row_mask:0xf bank_mask:0xf\n\tv_and_b32_dpp v23, v25, v63 quad_perm:[1,1,1,1] row_mask:0xf bank_mask:0xf\n\tv_mfma_f32_16x16x32_bf16 v[48:51], a[0:3], v[16:19], v[48:51]\n\tv_and_b32_dpp v16, v26, v60 quad_perm:[1,1,1,1] row_mask:0xf bank_mask:0xf\n\tv_and_b32_dpp v17, v26, v61 quad_perm:[1,1,1,1] row_mask:0xf bank_mask:0xf\n\tv_and_b32_dpp v18, v26, v62 quad_perm:[1,1,1,1] row_mask:0xf bank_mask:0xf\n\tv_and_b32_dpp v19, v26, v63 quad_perm:[1,1,1,1] row_mask:0xf bank_mask:0xf\n\tv_mfma_f32_16x16x32_bf16 v[48:51], a[4:7], v[20:23], v[48:51]\n\tv_and_b32_dpp v20, v27, v60 quad_perm:[1,1,1,1] row_mask:0xf bank_mask:0xf\n\tv_and_b32_dpp v21, v27, v61 quad_perm:[1,1,1,1] row_mask:0xf bank_mask:0xf\n\tv_and_b32_dpp v22, v27, v62 quad_perm:[1,1,1,1] row_mask:0xf bank_mask:0xf\n\tv_and_b32_dpp v23, v27, v63 quad_perm:[1,1,1,1] row_mask:0xf bank_mask:0xf\n\tv_mfma_f32_16x16x32_bf16 v[48:51], a[8:11], v[16:19], v[48:51]\n\tv_and_b32_dpp v16, v24, v60 quad_perm:[2,2,2,2] row_mask:0xf bank_mask:0xf\n\tv_and_b32_dpp v17, v24, v61 quad_perm:[2,2,2,2] row_mask:0xf bank_mask:0xf\n\tv_and_b32_dpp v18, v24, v62 quad_perm:[2,2,2,2] row_mask:0xf bank_mask:0xf\n\tv_and_b32_dpp v19, v24, v63 quad_perm:[2,2,2,2] row_mask:0xf bank_mask:0xf\n\tv_mfma_f32_16x16x32_bf16 v[48:51], a[12:15], v[20:23], v[48:51]\n\tv_and_b32_dpp v20, v25, v60 quad_perm:[2,2,2,2] row_mask:0xf bank_mask:0xf\n\tv_and_b32_dpp v21, v25, v61 quad_perm:[2,2,2,2] row_mask:0xf bank_mask:0xf\n\tv_and_b32_dpp v22, v25, v62 quad_perm:[2,2,2,2] row_mask:0xf bank_mask:0xf\n\tv_and_b32_dpp v23, v25, v63 quad_perm:[2,2,2,2] row_mask:0xf bank_mask:0xf\n\tv_mfma_f32_16x16x32_bf16 v[48:51], a[16:19], v[16:19], v[48:51]\n\tv_and_b32_dpp v16, v26, v60 quad_perm:[2,2,2,2] row_mask:0xf bank_mask:0xf\n\tv_and_b32_dpp v17, v26, v61 quad_perm:[2,2,2,2] row_mask:0xf bank_mask:0xf\n\tv_and_b32_dpp v18, v26, v62 quad_perm:[2,2,2,2] row_mask:0xf bank_mask:0xf\n\tv_and_b32_dpp v19, v26, v63 quad_perm:[2,2,2,2] row_mask:0xf bank_mask:0xf\n\tv_mfma_f32_16x16x32_bf16 v[48:51], a[20:23], v[20:23], v[48:51]\n\tv_and_b32_dpp v20, v27, v60 quad_perm:[2,2,2,2] row_mask:0xf bank_mask:0xf\n\tv_and_b32_dpp v21, v27, v61 quad_perm:[2,2,2,2] row_mask:0xf bank_mask:0xf\n\tv_and_b32_dpp v22, v27, v62 quad_perm:[2,2,2,2] row_mask:0xf bank_mask:0xf\n\tv_and_b32_dpp v23, v27, v63 quad_perm:[2,2,2,2] row_mask:0xf bank_mask:0xf\n\tv_mfma_f32_16x16x32_bf16 v[48:51], a[24:27], v[16:19], v[48:51]\n\tv_and_b32_dpp v16, v24, v60 quad_perm:[3,3,3,3] row_mask:0xf bank_mask:0xf\n\tv_and_b32_dpp v17, v24, v61 quad_perm:[3,3,3,3] row_mask:0xf bank_mask:0xf\n\tv_and_b32_dpp v18, v24, v62 quad_perm:[3,3,3,3] row_mask:0xf bank_mask:0xf\n\tv_and_b32_dpp v19, v24, v63 quad_perm:[3,3,3,3] row_mask:0xf bank_mask:0xf\n\tv_mfma_f32_16x16x32_bf16 v[48:51], a[28:31], v[20:23], v[48:51]\n\tv_and_b32_dpp v20, v25, v60 quad_perm:[3,3,3,3] row_mask:0xf bank_mask:0xf\n\tv_and_b32_dpp v21, v25, v61 quad_perm:[3,3,3,3] row_mask:0xf bank_mask:0xf\n\tv_and_b32_dpp v22, v25, v62 quad_perm:[3,3,3,3] row_mask:0xf bank_mask:0xf\n\tv_and_b32_dpp v23, v25, v63 quad_perm:[3,3,3,3] row_mask:0xf bank_mask:0xf\n\tv_mfma_f32_16x16x32_bf16 v[48:51], a[128:131], v[16:19], v[48:51]\n\tv_and_b32_dpp v16, v26, v60 quad_perm:[3,3,3,3] row_mask:0xf bank_mask:0xf\n\tv_and_b32_dpp v17, v26, v61 quad_perm:[3,3,3,3] row_mask:0xf bank_mask:0xf\n\tv_and_b32_dpp v18, v26, v62 quad_perm:[3,3,3,3] row_mask:0xf bank_mask:0xf\n\tv_and_b32_dpp v19, v26, v63 quad_perm:[3,3,3,3] row_mask:0xf bank_mask:0xf\n\tv_mfma_f32_16x16x32_bf16 v[48:51], a[132:135], v[20:23], v[48:51]\n\tv_and_b32_dpp v20, v27, v60 quad_perm:[3,3,3,3] row_mask:0xf bank_mask:0xf\n\tv_and_b32_dpp v21, v27, v61 quad_perm:[3,3,3,3] row_mask:0xf bank_mask:0xf\n\tv_and_b32_dpp v22, v27, v62 quad_perm:[3,3,3,3] row_mask:0xf bank_mask:0xf\n\tv_and_b32_dpp v23, v27, v63 quad_perm:[3,3,3,3] row_mask:0xf bank_mask:0xf\n\tv_mfma_f32_16x16x32_bf16 v[48:51], a[136:139], v[16:19], v[48:51]\n\tv_and_b32_dpp v16, v28, v60 quad_perm:[0,0,0,0] row_mask:0xf bank_mask:0xf\n\tv_and_b32_dpp v17, v28, v61 quad_perm:[0,0,0,0] row_mask:0xf bank_mask:0xf\n\tv_and_b32_dpp v18, v28, v62 quad_perm:[0,0,0,0] row_mask:0xf bank_mask:0xf\n\tv_and_b32_dpp v19, v28, v63 quad_perm:[0,0,0,0] row_mask:0xf bank_mask:0xf\n\tv_mfma_f32_16x16x32_bf16 v[48:51], a[140:143], v[20:23], v[48:51]\n\tv_and_b32_dpp v20, v29, v60 quad_perm:[0,0,0,0] row_mask:0xf bank_mask:0xf\n\tv_and_b32_dpp v21, v29, v61 quad_perm:[0,0,0,0] row_mask:0xf bank_mask:0xf\n\tv_and_b32_dpp v22, v29, v62 quad_perm:[0,0,0,0] row_mask:0xf bank_mask:0xf\n\tv_and_b32_dpp v23, v29, v63 quad_perm:[0,0,0,0] row_mask:0xf bank_mask:0xf\n\tv_mfma_f32_16x16x32_bf16 v[48:51], v[96:99], v[16:19], v[48:51]\n\tv_and_b32_dpp v16, v30, v60 quad_perm:[0,0,0,0] row_mask:0xf bank_mask:0xf\n\tv_and_b32_dpp v17, v30, v61 quad_perm:[0,0,0,0] row_mask:0xf bank_mask:0xf\n\tv_and_b32_dpp v18, v30, v62 quad_perm:[0,0,0,0] row_mask:0xf bank_mask:0xf\n\tv_and_b32_dpp v19, v30, v63 quad_perm:[0,0,0,0] row_mask:0xf bank_mask:0xf\n\tv_mfma_f32_16x16x32_bf16 v[48:51], v[100:103], v[20:23], v[48:51]\n\tv_and_b32_dpp v20, v31, v60 quad_perm:[0,0,0,0] row_mask:0xf bank_mask:0xf\n\tv_and_b32_dpp v21, v31, v61 quad_perm:[0,0,0,0] row_mask:0xf bank_mask:0xf\n\tv_and_b32_dpp v22, v31, v62 quad_perm:[0,0,0,0] row_mask:0xf bank_mask:0xf\n\tv_and_b32_dpp v23, v31, v63 quad_perm:[0,0,0,0] row_mask:0xf bank_mask:0xf\n\tv_mfma_f32_16x16x32_bf16 v[48:51], v[104:107], v[16:19], v[48:51]\n\tv_and_b32_dpp v16, v28, v60 quad_perm:[1,1,1,1] row_mask:0xf bank_mask:0xf\n\tv_and_b32_dpp v17, v28, v61 quad_perm:[1,1,1,1] row_mask:0xf bank_mask:0xf\n\tv_and_b32_dpp v18, v28, v62 quad_perm:[1,1,1,1] row_mask:0xf bank_mask:0xf\n\tv_and_b32_dpp v19, v28, v63 quad_perm:[1,1,1,1] row_mask:0xf bank_mask:0xf\n\tv_mfma_f32_16x16x32_bf16 v[48:51], v[108:111], v[20:23], v[48:51]\n\tv_and_b32_dpp v20, v29, v60 quad_perm:[1,1,1,1] row_mask:0xf bank_mask:0xf\n\tv_and_b32_dpp v21, v29, v61 quad_perm:[1,1,1,1] row_mask:0xf bank_mask:0xf\n\tv_and_b32_dpp v22, v29, v62 quad_perm:[1,1,1,1] row_mask:0xf bank_mask:0xf\n\tv_and_b32_dpp v23, v29, v63 quad_perm:[1,1,1,1] row_mask:0xf bank_mask:0xf\n\tv_mfma_f32_16x16x32_bf16 v[48:51], v[112:115], v[16:19], v[48:51]\n\tv_and_b32_dpp v16, v30, v60 quad_perm:[1,1,1,1] row_mask:0xf bank_mask:0xf\n\tv_and_b32_dpp v17, v30, v61 quad_perm:[1,1,1,1] row_mask:0xf bank_mask:0xf\n\tv_and_b32_dpp v18, v30, v62 quad_perm:[1,1,1,1] row_mask:0xf bank_mask:0xf\n\tv_and_b32_dpp v19, v30, v63 quad_perm:[1,1,1,1] row_mask:0xf bank_mask:0xf\n\tv_mfma_f32_16x16x32_bf16 v[48:51], v[116:119], v[20:23], v[48:51]\n\tv_and_b32_dpp v20, v31, v60 quad_perm:[1,1,1,1] row_mask:0xf bank_mask:0xf\n\tv_and_b32_dpp v21, v31, v61 quad_perm:[1,1,1,1] row_mask:0xf bank_mask:0xf\n\tv_and_b32_dpp v22, v31, v62 quad_perm:[1,1,1,1] row_mask:0xf bank_mask:0xf\n\tv_and_b32_dpp v23, v31, v63 quad_perm:[1,1,1,1] row_mask:0xf bank_mask:0xf\n\tv_mfma_f32_16x16x32_bf16 v[48:51], v[120:123], v[16:19], v[48:51]\n\tv_and_b32_dpp v16, v28, v60 quad_perm:[2,2,2,2] row_mask:0xf bank_mask:0xf\n\tv_and_b32_dpp v17, v28, v61 quad_perm:[2,2,2,2] row_mask:0xf bank_mask:0xf\n\tv_and_b32_dpp v18, v28, v62 quad_perm:[2,2,2,2] row_mask:0xf bank_mask:0xf\n\tv_and_b32_dpp v19, v28, v63 quad_perm:[2,2,2,2] row_mask:0xf bank_mask:0xf\n\tv_mfma_f32_16x16x32_bf16 v[48:51], v[124:127], v[20:23], v[48:51]\n\tv_and_b32_dpp v20, v29, v60 quad_perm:[2,2,2,2] row_mask:0xf bank_mask:0xf\n\tv_and_b32_dpp v21, v29, v61 quad_perm:[2,2,2,2] row_mask:0xf bank_mask:0xf\n\tv_and_b32_dpp v22, v29, v62 quad_perm:[2,2,2,2] row_mask:0xf bank_mask:0xf\n\tv_and_b32_dpp v23, v29, v63 quad_perm:[2,2,2,2] row_mask:0xf bank_mask:0xf\n\tv_mfma_f32_16x16x32_bf16 v[48:51], a[80:83], v[16:19], v[48:51]\n\tv_and_b32_dpp v16, v30, v60 quad_perm:[2,2,2,2] row_mask:0xf bank_mask:0xf\n\tv_and_b32_dpp v17, v30, v61 quad_perm:[2,2,2,2] row_mask:0xf bank_mask:0xf\n\tv_and_b32_dpp v18, v30, v62 quad_perm:[2,2,2,2] row_mask:0xf bank_mask:0xf\n\tv_and_b32_dpp v19, v30, v63 quad_perm:[2,2,2,2] row_mask:0xf bank_mask:0xf\n\tv_mfma_f32_16x16x32_bf16 v[48:51], a[84:87], v[20:23], v[48:51]\n\tv_and_b32_dpp v20, v31, v60 quad_perm:[2,2,2,2] row_mask:0xf bank_mask:0xf\n\tv_and_b32_dpp v21, v31, v61 quad_perm:[2,2,2,2] row_mask:0xf bank_mask:0xf\n\tv_and_b32_dpp v22, v31, v62 quad_perm:[2,2,2,2] row_mask:0xf bank_mask:0xf\n\tv_and_b32_dpp v23, v31, v63 quad_perm:[2,2,2,2] row_mask:0xf bank_mask:0xf\n\tv_mfma_f32_16x16x32_bf16 v[48:51], a[88:91], v[16:19], v[48:51]\n\tv_and_b32_dpp v16, v28, v60 quad_perm:[3,3,3,3] row_mask:0xf bank_mask:0xf\n\tv_and_b32_dpp v17, v28, v61 quad_perm:[3,3,3,3] row_mask:0xf bank_mask:0xf\n\tv_and_b32_dpp v18, v28, v62 quad_perm:[3,3,3,3] row_mask:0xf bank_mask:0xf\n\tv_and_b32_dpp v19, v28, v63 quad_perm:[3,3,3,3] row_mask:0xf bank_mask:0xf\n\tv_mfma_f32_16x16x32_bf16 v[48:51], a[92:95], v[20:23], v[48:51]\n\tv_and_b32_dpp v20, v29, v60 quad_perm:[3,3,3,3] row_mask:0xf bank_mask:0xf\n\tv_and_b32_dpp v21, v29, v61 quad_perm:[3,3,3,3] row_mask:0xf bank_mask:0xf\n\tv_and_b32_dpp v22, v29, v62 quad_perm:[3,3,3,3] row_mask:0xf bank_mask:0xf\n\tv_and_b32_dpp v23, v29, v63 quad_perm:[3,3,3,3] row_mask:0xf bank_mask:0xf\n\tv_mfma_f32_16x16x32_bf16 v[48:51], a[96:99], v[16:19], v[48:51]\n\tv_and_b32_dpp v16, v30, v60 quad_perm:[3,3,3,3] row_mask:0xf bank_mask:0xf\n\tv_and_b32_dpp v17, v30, v61 quad_perm:[3,3,3,3] row_mask:0xf bank_mask:0xf\n\tv_and_b32_dpp v18, v30, v62 quad_perm:[3,3,3,3] row_mask:0xf bank_mask:0xf\n\tv_and_b32_dpp v19, v30, v63 quad_perm:[3,3,3,3] row_mask:0xf bank_mask:0xf\n\tv_mfma_f32_16x16x32_bf16 v[48:51], a[100:103], v[20:23], v[48:51]\n\tv_and_b32_dpp v20, v31, v60 quad_perm:[3,3,3,3] row_mask:0xf bank_mask:0xf\n\tv_and_b32_dpp v21, v31, v61 quad_perm:[3,3,3,3] row_mask:0xf bank_mask:0xf\n\tv_and_b32_dpp v22, v31, v62 quad_perm:[3,3,3,3] row_mask:0xf bank_mask:0xf\n\tv_and_b32_dpp v23, v31, v63 quad_perm:[3,3,3,3] row_mask:0xf bank_mask:0xf\n\tv_mfma_f32_16x16x32_bf16 v[48:51], a[104:107], v[16:19], v[48:51]\n\tv_and_b32_dpp v16, v32, v60 quad_perm:[0,0,0,0] row_mask:0xf bank_mask:0xf\n\tv_and_b32_dpp v17, v32, v61 quad_perm:[0,0,0,0] row_mask:0xf bank_mask:0xf\n\tv_and_b32_dpp v18, v32, v62 quad_perm:[0,0,0,0] row_mask:0xf bank_mask:0xf\n\tv_and_b32_dpp v19, v32, v63 quad_perm:[0,0,0,0] row_mask:0xf bank_mask:0xf\n\tv_mfma_f32_16x16x32_bf16 v[48:51], a[108:111], v[20:23], v[48:51]\n\tv_and_b32_dpp v20, v33, v60 quad_perm:[0,0,0,0] row_mask:0xf bank_mask:0xf\n\tv_and_b32_dpp v21, v33, v61 quad_perm:[0,0,0,0] row_mask:0xf bank_mask:0xf\n\tv_and_b32_dpp v22, v33, v62 quad_perm:[0,0,0,0] row_mask:0xf bank_mask:0xf\n\tv_and_b32_dpp v23, v33, v63 quad_perm:[0,0,0,0] row_mask:0xf bank_mask:0xf\n\tv_mfma_f32_16x16x32_bf16 v[48:51], a[112:115], v[16:19], v[48:51]\n\tv_and_b32_dpp v16, v34, v60 quad_perm:[0,0,0,0] row_mask:0xf bank_mask:0xf\n\tv_and_b32_dpp v17, v34, v61 quad_perm:[0,0,0,0] row_mask:0xf bank_mask:0xf\n\tv_and_b32_dpp v18, v34, v62 quad_perm:[0,0,0,0] row_mask:0xf bank_mask:0xf\n\tv_and_b32_dpp v19, v34, v63 quad_perm:[0,0,0,0] row_mask:0xf bank_mask:0xf\n\tv_mfma_f32_16x16x32_bf16 v[48:51], a[116:119], v[20:23], v[48:51]\n\tv_and_b32_dpp v20, v35, v60 quad_perm:[0,0,0,0] row_mask:0xf bank_mask:0xf\n\tv_and_b32_dpp v21, v35, v61 quad_perm:[0,0,0,0] row_mask:0xf bank_mask:0xf\n\tv_and_b32_dpp v22, v35, v62 quad_perm:[0,0,0,0] row_mask:0xf bank_mask:0xf\n\tv_and_b32_dpp v23, v35, v63 quad_perm:[0,0,0,0] row_mask:0xf bank_mask:0xf\n\tv_mfma_f32_16x16x32_bf16 v[48:51], a[120:123], v[16:19], v[48:51]\n\tv_and_b32_dpp v16, v32, v60 quad_perm:[1,1,1,1] row_mask:0xf bank_mask:0xf\n\tv_and_b32_dpp v17, v32, v61 quad_perm:[1,1,1,1] row_mask:0xf bank_mask:0xf\n\tv_and_b32_dpp v18, v32, v62 quad_perm:[1,1,1,1] row_mask:0xf bank_mask:0xf\n\tv_and_b32_dpp v19, v32, v63 quad_perm:[1,1,1,1] row_mask:0xf bank_mask:0xf\n\tv_mfma_f32_16x16x32_bf16 v[48:51], a[124:127], v[20:23], v[48:51]\n\tv_and_b32_dpp v20, v33, v60 quad_perm:[1,1,1,1] row_mask:0xf bank_mask:0xf\n\tv_and_b32_dpp v21, v33, v61 quad_perm:[1,1,1,1] row_mask:0xf bank_mask:0xf\n\tv_and_b32_dpp v22, v33, v62 quad_perm:[1,1,1,1] row_mask:0xf bank_mask:0xf\n\tv_and_b32_dpp v23, v33, v63 quad_perm:[1,1,1,1] row_mask:0xf bank_mask:0xf\n\tv_mfma_f32_16x16x32_bf16 v[48:51], a[144:147], v[16:19], v[48:51]\n\tv_and_b32_dpp v16, v34, v60 quad_perm:[1,1,1,1] row_mask:0xf bank_mask:0xf\n\tv_and_b32_dpp v17, v34, v61 quad_perm:[1,1,1,1] row_mask:0xf bank_mask:0xf\n\tv_and_b32_dpp v18, v34, v62 quad_perm:[1,1,1,1] row_mask:0xf bank_mask:0xf\n\tv_and_b32_dpp v19, v34, v63 quad_perm:[1,1,1,1] row_mask:0xf bank_mask:0xf\n\tv_mfma_f32_16x16x32_bf16 v[48:51], a[148:151], v[20:23], v[48:51]\n\tv_and_b32_dpp v20, v35, v60 quad_perm:[1,1,1,1] row_mask:0xf bank_mask:0xf\n\tv_and_b32_dpp v21, v35, v61 quad_perm:[1,1,1,1] row_mask:0xf bank_mask:0xf\n\tv_and_b32_dpp v22, v35, v62 quad_perm:[1,1,1,1] row_mask:0xf bank_mask:0xf\n\tv_and_b32_dpp v23, v35, v63 quad_perm:[1,1,1,1] row_mask:0xf bank_mask:0xf\n\tv_mfma_f32_16x16x32_bf16 v[48:51], a[152:155], v[16:19], v[48:51]\n\tv_and_b32_dpp v16, v32, v60 quad_perm:[2,2,2,2] row_mask:0xf bank_mask:0xf\n\tv_and_b32_dpp v17, v32, v61 quad_perm:[2,2,2,2] row_mask:0xf bank_mask:0xf\n\tv_and_b32_dpp v18, v32, v62 quad_perm:[2,2,2,2] row_mask:0xf bank_mask:0xf\n\tv_and_b32_dpp v19, v32, v63 quad_perm:[2,2,2,2] row_mask:0xf bank_mask:0xf\n\tv_mfma_f32_16x16x32_bf16 v[48:51], a[156:159], v[20:23], v[48:51]\n\tv_and_b32_dpp v20, v33, v60 quad_perm:[2,2,2,2] row_mask:0xf bank_mask:0xf\n\tv_and_b32_dpp v21, v33, v61 quad_perm:[2,2,2,2] row_mask:0xf bank_mask:0xf\n\tv_and_b32_dpp v22, v33, v62 quad_perm:[2,2,2,2] row_mask:0xf bank_mask:0xf\n\tv_and_b32_dpp v23, v33, v63 quad_perm:[2,2,2,2] row_mask:0xf bank_mask:0xf\n\tv_mfma_f32_16x16x32_bf16 v[48:51], v[192:195], v[16:19], v[48:51]\n\tv_and_b32_dpp v16, v34, v60 quad_perm:[2,2,2,2] row_mask:0xf bank_mask:0xf\n\tv_and_b32_dpp v17, v34, v61 quad_perm:[2,2,2,2] row_mask:0xf bank_mask:0xf\n\tv_and_b32_dpp v18, v34, v62 quad_perm:[2,2,2,2] row_mask:0xf bank_mask:0xf\n\tv_and_b32_dpp v19, v34, v63 quad_perm:[2,2,2,2] row_mask:0xf bank_mask:0xf\n\tv_mfma_f32_16x16x32_bf16 v[48:51], v[196:199], v[20:23], v[48:51]\n\tv_and_b32_dpp v20, v35, v60 quad_perm:[2,2,2,2] row_mask:0xf bank_mask:0xf\n\tv_and_b32_dpp v21, v35, v61 quad_perm:[2,2,2,2] row_mask:0xf bank_mask:0xf\n\tv_and_b32_dpp v22, v35, v62 quad_perm:[2,2,2,2] row_mask:0xf bank_mask:0xf\n\tv_and_b32_dpp v23, v35, v63 quad_perm:[2,2,2,2] row_mask:0xf bank_mask:0xf\n\tv_mfma_f32_16x16x32_bf16 v[48:51], v[200:203], v[16:19], v[48:51]\n\tv_and_b32_dpp v16, v32, v60 quad_perm:[3,3,3,3] row_mask:0xf bank_mask:0xf\n\tv_and_b32_dpp v17, v32, v61 quad_perm:[3,3,3,3] row_mask:0xf bank_mask:0xf\n\tv_and_b32_dpp v18, v32, v62 quad_perm:[3,3,3,3] row_mask:0xf bank_mask:0xf\n\tv_and_b32_dpp v19, v32, v63 quad_perm:[3,3,3,3] row_mask:0xf bank_mask:0xf\n\tv_mfma_f32_16x16x32_bf16 v[48:51], v[204:207], v[20:23], v[48:51]\n\tv_and_b32_dpp v20, v33, v60 quad_perm:[3,3,3,3] row_mask:0xf bank_mask:0xf\n\tv_and_b32_dpp v21, v33, v61 quad_perm:[3,3,3,3] row_mask:0xf bank_mask:0xf\n\tv_and_b32_dpp v22, v33, v62 quad_perm:[3,3,3,3] row_mask:0xf bank_mask:0xf\n\tv_and_b32_dpp v23, v33, v63 quad_perm:[3,3,3,3] row_mask:0xf bank_mask:0xf\n\tv_mfma_f32_16x16x32_bf16 v[48:51], v[208:211], v[16:19], v[48:51]\n\tv_and_b32_dpp v16, v34, v60 quad_perm:[3,3,3,3] row_mask:0xf bank_mask:0xf\n\tv_and_b32_dpp v17, v34, v61 quad_perm:[3,3,3,3] row_mask:0xf bank_mask:0xf\n\tv_and_b32_dpp v18, v34, v62 quad_perm:[3,3,3,3] row_mask:0xf bank_mask:0xf\n\tv_and_b32_dpp v19, v34, v63 quad_perm:[3,3,3,3] row_mask:0xf bank_mask:0xf\n\tv_mfma_f32_16x16x32_bf16 v[48:51], v[212:215], v[20:23], v[48:51]\n\tv_and_b32_dpp v20, v35, v60 quad_perm:[3,3,3,3] row_mask:0xf bank_mask:0xf\n\tv_and_b32_dpp v21, v35, v61 quad_perm:[3,3,3,3] row_mask:0xf bank_mask:0xf\n\tv_and_b32_dpp v22, v35, v62 quad_perm:[3,3,3,3] row_mask:0xf bank_mask:0xf\n\tv_and_b32_dpp v23, v35, v63 quad_perm:[3,3,3,3] row_mask:0xf bank_mask:0xf\n\tv_mfma_f32_16x16x32_bf16 v[48:51], v[216:219], v[16:19], v[48:51]\n\tv_and_b32_dpp v16, v36, v60 quad_perm:[0,0,0,0] row_mask:0xf bank_mask:0xf\n\tv_and_b32_dpp v17, v36, v61 quad_perm:[0,0,0,0] row_mask:0xf bank_mask:0xf\n\tv_and_b32_dpp v18, v36, v62 quad_perm:[0,0,0,0] row_mask:0xf bank_mask:0xf\n\tv_and_b32_dpp v19, v36, v63 quad_perm:[0,0,0,0] row_mask:0xf bank_mask:0xf\n\tv_mfma_f32_16x16x32_bf16 v[48:51], v[220:223], v[20:23], v[48:51]\n\tv_and_b32_dpp v20, v37, v60 quad_perm:[0,0,0,0] row_mask:0xf bank_mask:0xf\n\tv_and_b32_dpp v21, v37, v61 quad_perm:[0,0,0,0] row_mask:0xf bank_mask:0xf\n\tv_and_b32_dpp v22, v37, v62 quad_perm:[0,0,0,0] row_mask:0xf bank_mask:0xf\n\tv_and_b32_dpp v23, v37, v63 quad_perm:[0,0,0,0] row_mask:0xf bank_mask:0xf\n\tv_mfma_f32_16x16x32_bf16 v[48:51], v[224:227], v[16:19], v[48:51]\n\tv_and_b32_dpp v16, v38, v60 quad_perm:[0,0,0,0] row_mask:0xf bank_mask:0xf\n\tv_and_b32_dpp v17, v38, v61 quad_perm:[0,0,0,0] row_mask:0xf bank_mask:0xf\n\tv_and_b32_dpp v18, v38, v62 quad_perm:[0,0,0,0] row_mask:0xf bank_mask:0xf\n\tv_and_b32_dpp v19, v38, v63 quad_perm:[0,0,0,0] row_mask:0xf bank_mask:0xf\n\tv_mfma_f32_16x16x32_bf16 v[48:51], v[228:231], v[20:23], v[48:51]\n\tv_and_b32_dpp v20, v39, v60 quad_perm:[0,0,0,0] row_mask:0xf bank_mask:0xf\n\tv_and_b32_dpp v21, v39, v61 quad_perm:[0,0,0,0] row_mask:0xf bank_mask:0xf\n\tv_and_b32_dpp v22, v39, v62 quad_perm:[0,0,0,0] row_mask:0xf bank_mask:0xf\n\tv_and_b32_dpp v23, v39, v63 quad_perm:[0,0,0,0] row_mask:0xf bank_mask:0xf\n\tv_mfma_f32_16x16x32_bf16 v[48:51], v[232:235], v[16:19], v[48:51]\n\tv_and_b32_dpp v16, v36, v60 quad_perm:[1,1,1,1] row_mask:0xf bank_mask:0xf\n\tv_and_b32_dpp v17, v36, v61 quad_perm:[1,1,1,1] row_mask:0xf bank_mask:0xf\n\tv_and_b32_dpp v18, v36, v62 quad_perm:[1,1,1,1] row_mask:0xf bank_mask:0xf\n\tv_and_b32_dpp v19, v36, v63 quad_perm:[1,1,1,1] row_mask:0xf bank_mask:0xf\n\tv_mfma_f32_16x16x32_bf16 v[48:51], v[236:239], v[20:23], v[48:51]\n\tv_and_b32_dpp v20, v37, v60 quad_perm:[1,1,1,1] row_mask:0xf bank_mask:0xf\n\tv_and_b32_dpp v21, v37, v61 quad_perm:[1,1,1,1] row_mask:0xf bank_mask:0xf\n\tv_and_b32_dpp v22, v37, v62 quad_perm:[1,1,1,1] row_mask:0xf bank_mask:0xf\n\tv_and_b32_dpp v23, v37, v63 quad_perm:[1,1,1,1] row_mask:0xf bank_mask:0xf\n\tv_mfma_f32_16x16x32_bf16 v[48:51], v[64:67], v[16:19], v[48:51]\n\tv_and_b32_dpp v16, v38, v60 quad_perm:[1,1,1,1] row_mask:0xf bank_mask:0xf\n\tv_and_b32_dpp v17, v38, v61 quad_perm:[1,1,1,1] row_mask:0xf bank_mask:0xf\n\tv_and_b32_dpp v18, v38, v62 quad_perm:[1,1,1,1] row_mask:0xf bank_mask:0xf\n\tv_and_b32_dpp v19, v38, v63 quad_perm:[1,1,1,1] row_mask:0xf bank_mask:0xf\n\tv_mfma_f32_16x16x32_bf16 v[48:51], v[68:71], v[20:23], v[48:51]\n\tv_and_b32_dpp v20, v39, v60 quad_perm:[1,1,1,1] row_mask:0xf bank_mask:0xf\n\tv_and_b32_dpp v21, v39, v61 quad_perm:[1,1,1,1] row_mask:0xf bank_mask:0xf\n\tv_and_b32_dpp v22, v39, v62 quad_perm:[1,1,1,1] row_mask:0xf bank_mask:0xf\n\tv_and_b32_dpp v23, v39, v63 quad_perm:[1,1,1,1] row_mask:0xf bank_mask:0xf\n\tv_mfma_f32_16x16x32_bf16 v[48:51], v[72:75], v[16:19], v[48:51]\n\tv_and_b32_dpp v16, v36, v60 quad_perm:[2,2,2,2] row_mask:0xf bank_mask:0xf\n\tv_and_b32_dpp v17, v36, v61 quad_perm:[2,2,2,2] row_mask:0xf bank_mask:0xf\n\tv_and_b32_dpp v18, v36, v62 quad_perm:[2,2,2,2] row_mask:0xf bank_mask:0xf\n\tv_and_b32_dpp v19, v36, v63 quad_perm:[2,2,2,2] row_mask:0xf bank_mask:0xf\n\tv_mfma_f32_16x16x32_bf16 v[48:51], v[76:79], v[20:23], v[48:51]\n\tv_and_b32_dpp v20, v37, v60 quad_perm:[2,2,2,2] row_mask:0xf bank_mask:0xf\n\tv_and_b32_dpp v21, v37, v61 quad_perm:[2,2,2,2] row_mask:0xf bank_mask:0xf\n\tv_and_b32_dpp v22, v37, v62 quad_perm:[2,2,2,2] row_mask:0xf bank_mask:0xf\n\tv_and_b32_dpp v23, v37, v63 quad_perm:[2,2,2,2] row_mask:0xf bank_mask:0xf\n\tv_mfma_f32_16x16x32_bf16 v[48:51], v[160:163], v[16:19], v[48:51]\n\tv_and_b32_dpp v16, v38, v60 quad_perm:[2,2,2,2] row_mask:0xf bank_mask:0xf\n\tv_and_b32_dpp v17, v38, v61 quad_perm:[2,2,2,2] row_mask:0xf bank_mask:0xf\n\tv_and_b32_dpp v18, v38, v62 quad_perm:[2,2,2,2] row_mask:0xf bank_mask:0xf\n\tv_and_b32_dpp v19, v38, v63 quad_perm:[2,2,2,2] row_mask:0xf bank_mask:0xf\n\tv_mfma_f32_16x16x32_bf16 v[48:51], v[164:167], v[20:23], v[48:51]\n\tv_and_b32_dpp v20, v39, v60 quad_perm:[2,2,2,2] row_mask:0xf bank_mask:0xf\n\tv_and_b32_dpp v21, v39, v61 quad_perm:[2,2,2,2] row_mask:0xf bank_mask:0xf\n\tv_and_b32_dpp v22, v39, v62 quad_perm:[2,2,2,2] row_mask:0xf bank_mask:0xf\n\tv_and_b32_dpp v23, v39, v63 quad_perm:[2,2,2,2] row_mask:0xf bank_mask:0xf\n\tv_mfma_f32_16x16x32_bf16 v[48:51], v[168:171], v[16:19], v[48:51]\n\tv_and_b32_dpp v16, v36, v60 quad_perm:[3,3,3,3] row_mask:0xf bank_mask:0xf\n\tv_and_b32_dpp v17, v36, v61 quad_perm:[3,3,3,3] row_mask:0xf bank_mask:0xf\n\tv_and_b32_dpp v18, v36, v62 quad_perm:[3,3,3,3] row_mask:0xf bank_mask:0xf\n\tv_and_b32_dpp v19, v36, v63 quad_perm:[3,3,3,3] row_mask:0xf bank_mask:0xf\n\tv_mfma_f32_16x16x32_bf16 v[48:51], v[172:175], v[20:23], v[48:51]\n\tv_and_b32_dpp v20, v37, v60 quad_perm:[3,3,3,3] row_mask:0xf bank_mask:0xf\n\tv_and_b32_dpp v21, v37, v61 quad_perm:[3,3,3,3] row_mask:0xf bank_mask:0xf\n\tv_and_b32_dpp v22, v37, v62 quad_perm:[3,3,3,3] row_mask:0xf bank_mask:0xf\n\tv_and_b32_dpp v23, v37, v63 quad_perm:[3,3,3,3] row_mask:0xf bank_mask:0xf\n\tv_mfma_f32_16x16x32_bf16 v[48:51], v[176:179], v[16:19], v[48:51]\n\tv_and_b32_dpp v16, v38, v60 quad_perm:[3,3,3,3] row_mask:0xf bank_mask:0xf\n\tv_and_b32_dpp v17, v38, v61 quad_perm:[3,3,3,3] row_mask:0xf bank_mask:0xf\n\tv_and_b32_dpp v18, v38, v62 quad_perm:[3,3,3,3] row_mask:0xf bank_mask:0xf\n\tv_and_b32_dpp v19, v38, v63 quad_perm:[3,3,3,3] row_mask:0xf bank_mask:0xf\n\tv_mfma_f32_16x16x32_bf16 v[48:51], v[180:183], v[20:23], v[48:51]\n\tv_and_b32_dpp v20, v39, v60 quad_perm:[3,3,3,3] row_mask:0xf bank_mask:0xf\n\tv_and_b32_dpp v21, v39, v61 quad_perm:[3,3,3,3] row_mask:0xf bank_mask:0xf\n\tv_and_b32_dpp v22, v39, v62 quad_perm:[3,3,3,3] row_mask:0xf bank_mask:0xf\n\tv_and_b32_dpp v23, v39, v63 quad_perm:[3,3,3,3] row_mask:0xf bank_mask:0xf\n\tv_mfma_f32_16x16x32_bf16 v[48:51], v[184:187], v[16:19], v[48:51]\n\ts_nop 0\n\tv_mfma_f32_16x16x32_bf16 v[48:51], v[188:191], v[20:23], v[48:51]\n\ts_nop 7\n\ts_nop 0\n\tds_write2_b32 v13, v48, v49 offset1:8\n\tds_write2_b32 v13, v50, v51 offset0:16 offset1:24" :  : "s"(CSC), "s"(MRG + w*1024u), "s"(MLB + w*8u) : "memory","scc","vcc","s98","s99","v4","v5","v6","v7","v8","v9","v10","v11","v12","v13","v14","v15","v16","v17","v18","v19","v20","v21","v22","v23","v24","v25","v26","v27","v28","v29","v30","v31","v32","v33","v34","v35","v36","v37","v38","v39","v40","v41","v42","v43");
  tref = merge_publish(w, s, wsb + 557056ull + (u64)kvh*10240u);
  {
  u32 ln = LANE(); u32 po = (2*xg + (w>>1))*10240u + (w&1)*4096u + gpos(ln>>4, ln&15u); u32 pm = (2*xg + (w>>1))*10240u + 8192u + (ln&15u)*16u + (w&1)*8u;
  u64 pb = wsb + 557056ull; u32x4 a0,a1,a2,a3; u32x2 ml;
  u32 bpa = ln & 48u, rda = RED + ln*16u - (ln>>5)*256u + w*4u; u64 yd = pin(wsb + 782336ull + (u64)(xg*2560 + i32*80));
  asm volatile("" : "+v"(po), "+v"(pm), "+v"(bpa), "+v"(rda));
  pace(tref, 37);
  asm volatile("global_load_dwordx4 %0, %5, %7 offset:0 \n\tglobal_load_dwordx4 %1, %5, %7 offset:1024 \n\tglobal_load_dwordx4 %2, %5, %7 offset:2048 \n\tglobal_load_dwordx4 %3, %5, %7 offset:3072 \n\tglobal_load_dwordx2 %4, %6, %7 \n\ts_waitcnt vmcnt(0)" : "=&v"(a0),"=&v"(a1),"=&v"(a2),"=&v"(a3),"=&v"(ml) : "v"(po), "v"(pm), "s"(pb) : "memory","scc","v43","v42","s98","s99");
  _Pragma("unroll 1") for (int sp = 0; sp < SPIN; sp++) { u32 mx = umax3(umax4(a0), umax4(a1), umax3(umax4(a2), umax4(a3), max(ml[0], ml[1]))); if (!__any(mx == 0xFFFFFFFFu)) break; __builtin_amdgcn_s_sleep(1);
  asm volatile("global_load_dwordx4 %0, %5, %7 offset:0 sc1\n\tglobal_load_dwordx4 %1, %5, %7 offset:1024 sc1\n\tglobal_load_dwordx4 %2, %5, %7 offset:2048 sc1\n\tglobal_load_dwordx4 %3, %5, %7 offset:3072 sc1\n\tglobal_load_dwordx2 %4, %6, %7 sc1\n\ts_waitcnt vmcnt(0)" : "=&v"(a0),"=&v"(a1),"=&v"(a2),"=&v"(a3),"=&v"(ml) : "v"(po), "v"(pm), "s"(pb) : "memory","scc","v43","v42","s98","s99");
  }
  u32 t0_, t1_, t2_, t3_, t4_, t5_;
  asm volatile("s_mov_b32 vcc_lo, 0x30003\n\ts_mov_b32 vcc_hi, 0x30003\n\tv_max_f32_dpp %0, %10, %10 row_ror:8 row_mask:0xf bank_mask:0xf\n\tv_mfma_f32_16x16x32_bf16 v[28:31], %6, a[208:211], 0\n\tv_cndmask_b32 v36, 0, v52, vcc\n\tv_cndmask_b32 v37, 0, v53, vcc\n\tv_max_f32_dpp %0, %0, %0 row_ror:4 row_mask:0xf bank_mask:0xf\n\tv_cndmask_b32 v38, 0, v54, vcc\n\tv_cndmask_b32 v39, 0, v55, vcc\n\ts_lshl_b64 vcc, vcc, 2\n\tv_max_f32_dpp %0, %0, %0 row_ror:2 row_mask:0xf bank_mask:0xf\n\tv_cndmask_b32 v40, 0, v52, vcc\n\tv_cndmask_b32 v41, 0, v53, vcc\n\tv_max_f32_dpp %0, %0, %0 row_ror:1 row_mask:0xf bank_mask:0xf\n\tv_mfma_f32_16x16x32_bf16 v[32:35], %6, v[36:39], 0\n\tv_sub_f32 %0, %10, %0\n\tv_cndmask_b32 v42, 0, v54, vcc\n\tv_exp_f32 %0, %0\n\tv_cndmask_b32 v43, 0, v55, vcc\n\ts_lshl_b64 vcc, vcc, 2\n\tv_mfma_f32_16x16x32_bf16 v[28:31], %7, a[212:215], v[28:31]\n\tv_mul_f32 %0, %0, %11\n\tv_cndmask_b32 v36, 0, v52, vcc\n\tds_bpermute_b32 %2, %12, %0 offset:0\n\tds_bpermute_b32 %3, %12, %0 offset:4\n\tds_bpermute_b32 %4, %12, %0 offset:8\n\tds_bpermute_b32 %5, %12, %0 offset:12\n\tv_mfma_f32_16x16x32_bf16 v[32:35], %7, v[40:43], v[32:35]\n\tv_add_f32_dpp %1, %0, %0 row_ror:8 row_mask:0xf bank_mask:0xf\n\tv_cndmask_b32 v37, 0, v53, vcc\n\tv_cndmask_b32 v38, 0, v54, vcc\n\tv_add_f32_dpp %1, %1, %1 row_ror:4 row_mask:0xf bank_mask:0xf\n\tv_cndmask_b32 v39, 0, v55, vcc\n\ts_lshl_b64 vcc, vcc, 2\n\tv_mfma_f32_16x16x32_bf16 v[28:31], %8, a[216:219], v[28:31]\n\tv_add_f32_dpp %1, %1, %1 row_ror:2 row_mask:0xf bank_mask:0xf\n\tv_cndmask_b32 v40, 0, v52, vcc\n\tv_mfma_f32_16x16x32_bf16 v[32:35], %8, v[36:39], v[32:35]\n\tv_add_f32_dpp %1, %1, %1 row_ror:1 row_mask:0xf bank_mask:0xf\n\tv_cndmask_b32 v41, 0, v53, vcc\n\tv_cndmask_b32 v42, 0, v54, vcc\n\tv_cndmask_b32 v43, 0, v55, vcc\n\tv_mfma_f32_16x16x32_bf16 v[28:31], %9, a[220:223], v[28:31]\n\tv_rcp_f32 %1, %1\n\tv_mfma_f32_16x16x32_bf16 v[32:35], %9, v[40:43], v[32:35]\n\ts_nop 5\n\ts_waitcnt lgkmcnt(0)\n\tv_mul_f32 %0, %2, v28\n\tv_fmac_f32 %0, %3, v29\n\tv_fmac_f32 %0, %4, v30\n\tv_fmac_f32 %0, %5, v31\n\tv_mul_f32 %2, %2, v32\n\tv_fmac_f32 %2, %3, v33\n\tv_fmac_f32 %2, %4, v34\n\tv_fmac_f32 %2, %5, v35\n\ts_nop 1\n\tv_permlane32_swap_b32 %0, %2\n\ts_nop 0\n\tv_add_f32 %0, %0, %2\n\tv_mov_b32 %2, %0\n\ts_nop 1\n\tv_permlane16_swap_b32 %0, %2\n\ts_nop 0\n\tv_add_f32 %0, %0, %2\n\ts_nop 1\n\tv_add_f32_dpp %0, %0, %0 row_shl:2 row_mask:0x4 bank_mask:0xf\n\ts_nop 1\n\tv_add_f32_dpp %0, %0, %0 row_shl:4 row_mask:0x4 bank_mask:0xf\n\ts_mov_b32 exec_lo, 0xffff\n\ts_mov_b32 exec_hi, 3\n\tv_mul_f32 %0, %0, %1\n\tds_write_b32 %13, %0\n\ts_mov_b64 exec, -1\n\ts_waitcnt lgkmcnt(0)\n\ts_barrier" : "=&v"(t0_),"=&v"(t1_),"=&v"(t2_),"=&v"(t3_),"=&v"(t4_),"=&v"(t5_) : "v"(a0),"v"(a1),"v"(a2),"v"(a3),"v"(ml[0]),"v"(ml[1]),"v"(bpa),"v"(rda) : "memory","scc","vcc","v28","v29","v30","v31","v32","v33","v34","v35","v36","v37","v38","v39","v40","v41","v42","v43");
  tref = now32(); wo_finish(ln, w, yd); }
  if (wg < 2) { u32 fw = wg, xr = 10368u - fw*1152u;
  {
  u32 ln = LANE(); u32 l40 = min(ln, 39u); u32 yo = w*640u + l40*16u; u64 yb = wsb + (u64)(784896u - fw*40960u), yb2 = yb + 7680ull, yb3 = yb + 15360ull;
  u32 xa = xr + (8u*w + l40/5u)*36u + (l40%5u)*8u; u32 x01 = *LDSP(u32, xa), x23 = *LDSP(u32, xa + 4);
  u32x4 y0,y1,y2,y3,y4,y5,y6,y7; u32 xb = l40 % 5u != 4u ? xa + 4u : ONES + 128u; f32x2 x0 = up2(x01), x2 = up2(x23);
  yb = pin(yb); yb2 = pin(yb2); yb3 = pin(yb3); asm volatile("" : "+v"(yo), "+v"(xa), "+v"(xb), "+v"(x0), "+v"(x2));
  pace(tref, 30);
  asm volatile("global_load_dwordx4 %0, %8, %9 offset:-2560 sc1\n\tglobal_load_dwordx4 %1, %8, %9 offset:0 sc1\n\tglobal_load_dwordx4 %2, %8, %9 offset:2560 sc1\n\tglobal_load_dwordx4 %3, %8, %10 offset:-2560 sc1\n\tglobal_load_dwordx4 %4, %8, %10 offset:0 sc1\n\tglobal_load_dwordx4 %5, %8, %10 offset:2560 sc1\n\tglobal_load_dwordx4 %6, %8, %11 offset:-2560 sc1\n\tglobal_load_dwordx4 %7, %8, %11 offset:0 sc1\n\ts_waitcnt vmcnt(0)" : "=&v"(y0),"=&v"(y1),"=&v"(y2),"=&v"(y3),"=&v"(y4),"=&v"(y5),"=&v"(y6),"=&v"(y7) : "v"(yo), "s"(yb), "s"(yb2), "s"(yb3) : "memory","scc","v43","v42","s98","s99");
  f32x4 ys = ((F4(y0)+F4(y1))+(F4(y2)+F4(y3))) + ((F4(y4)+F4(y5))+(F4(y6)+F4(y7))); if (__any(__builtin_isunordered(ys[0], ys[1]) || __builtin_isunordered(ys[2], ys[3]))) {
  _Pragma("unroll 1") for (int sp = 0; sp < SPIN; sp++) {
  asm volatile("global_load_dwordx4 %0, %8, %9 offset:-2560 sc1\n\tglobal_load_dwordx4 %1, %8, %9 offset:0 sc1\n\tglobal_load_dwordx4 %2, %8, %9 offset:2560 sc1\n\tglobal_load_dwordx4 %3, %8, %10 offset:-2560 sc1\n\tglobal_load_dwordx4 %4, %8, %10 offset:0 sc1\n\tglobal_load_dwordx4 %5, %8, %10 offset:2560 sc1\n\tglobal_load_dwordx4 %6, %8, %11 offset:-2560 sc1\n\tglobal_load_dwordx4 %7, %8, %11 offset:0 sc1\n\ts_waitcnt vmcnt(0)" : "=&v"(y0),"=&v"(y1),"=&v"(y2),"=&v"(y3),"=&v"(y4),"=&v"(y5),"=&v"(y6),"=&v"(y7) : "v"(yo), "s"(yb), "s"(yb2), "s"(yb3) : "memory","scc","v43","v42","s98","s99");
  u32 mx = umax3(umax3(umax4(y0), umax4(y1), umax4(y2)), umax3(umax4(y3), umax4(y4), umax4(y5)), max(umax4(y6), umax4(y7))); if (!__any(mx == 0xFFFFFFFFu)) break; __builtin_amdgcn_s_sleep(1); }
  ys = ((F4(y0)+F4(y1))+(F4(y2)+F4(y3))) + ((F4(y4)+F4(y5))+(F4(y6)+F4(y7))); }
  *LDSP(u32, xa) = pkv(x0 + f32x2{ys[0], ys[1]}); *LDSP(u32, xb) = pkv(x2 + f32x2{ys[2], ys[3]});
  BAR(); }
  if (w == 0) { u32 ln = LANE(); u32x4 xp; u32 xt;
  mixer(xr, ln, xp, xt);
  char* op = outp + (1152u - fw*1152u); *(u32x4*)(op + ln*16u) = xp; *(u16*)(op + 1024 + ln*2u) = (u16)xt; } }

  asm volatile(".Lcode_end:");
}

extern "C" void kernel_launch(void* const* d_in, const int* in_sizes, int n_in, void* d_out, int out_size,
                              void* d_ws, size_t ws_size, hipStream_t stream) {
  (void)hipFuncSetAttribute((const void*)fused_kernel, hipFuncAttributeMaxDynamicSharedMemorySize, 163840);
  fused_kernel<<<256, 256, 163840, stream>>>((const char*)d_in[0], (const char*)d_in[1], (const char*)d_in[2], (const char*)d_in[3],
                                        (const char*)d_in[4], (const u32*)d_in[6], (const u32*)d_in[7], (const u32*)d_in[8],
                                        (const char*)d_in[9], (char*)d_out, (char*)d_ws);
}
```
